```python
import torch

def _softmax(x, axis=-1):
    return torch.softmax(x, dim=axis)
def _gelu(x):
    return torch.nn.functional.gelu(x, approximate="tanh")
def _silu(x):
    return torch.nn.functional.silu(x)
def _sigmoid(x):
    return torch.sigmoid(x)
def _layernorm(x, axis=-1, eps=1e-5):
    m = x.mean(dim=axis, keepdim=True)
    v = x.var(dim=axis, keepdim=True, unbiased=False)
    return (x - m) / torch.sqrt(v + eps)
def _scatter_add(tgt, idx, src, axis=0):
    return tgt.clone().index_add_(axis, idx, src)
def _segment_sum(src, seg, n):
    out = torch.zeros((n,) + src.shape[1:], dtype=src.dtype)
    return out.index_add_(0, seg, src)
def _pad(x, pw):
    flat = tuple(v for pair in reversed(pw) for v in pair)
    return torch.nn.functional.pad(x, flat)

kernel_name = 'concat_heads__sliding_block_cached__gqa_block_cached__decode'

def setup_inputs():

    _w = (lambda a: (lambda t: t.to(torch.bfloat16) if t.is_floating_point() else t)(torch.as_tensor(a)))
    def _A_setup():
        B, Sq, D = 2, 1, 512
        Hq, Hkv, Sk, Dh = 32, 8, 8192, 128
        assert Hq % Hkv == 0, (Hq, Hkv)
        return {
            "x": _w((lambda *s: torch.randn(*s, dtype=torch.bfloat16) * 0.2)(B, Sq, D)),
            "k_cache": _w((lambda *s: torch.randn(*s, dtype=torch.bfloat16) * 3.2)(B, Hkv, Sk, Dh)),
            "v_cache": _w((lambda *s: torch.randn(*s, dtype=torch.bfloat16) * 1.6)(B, Hkv, Sk, Dh)),
            "wq": _w((lambda *s: torch.randn(*s, dtype=torch.bfloat16) * 3.2)(D, Hq * Dh) * (D ** -0.5)),
            "wo": _w((lambda *s: torch.randn(*s, dtype=torch.bfloat16) * 1.6)(Hq * Dh, D) * ((Hq * Dh) ** -0.5)),
        }
    _A_in = _A_setup()

    def _B_setup():
        B, Sq, D = 2, 1, 512
        Hq, Hkv, Sk, Dh = 32, 8, 8192, 128
        assert Hq % Hkv == 0, (Hq, Hkv)
        return {
            "x": _w((lambda *s: torch.randn(*s, dtype=torch.bfloat16) * 0.4)(B, Sq, D)),
            "k_cache": _w((lambda *s: torch.randn(*s, dtype=torch.bfloat16) * 3.2)(B, Hkv, Sk, Dh)),
            "v_cache": _w((lambda *s: torch.randn(*s, dtype=torch.bfloat16) * 0.8)(B, Hkv, Sk, Dh)),
            "wq": _w((lambda *s: torch.randn(*s, dtype=torch.bfloat16) * 3.2)(D, Hq * Dh) * (D ** -0.5)),
            "wo": _w((lambda *s: torch.randn(*s, dtype=torch.bfloat16) * 0.8)(Hq * Dh, D) * ((Hq * Dh) ** -0.5)),
        }
    _B_in = _B_setup()

    return {
        "A_x": _A_in["x"],
        "A_k_cache": _A_in["k_cache"],
        "A_v_cache": _A_in["v_cache"],
        "A_wq": _A_in["wq"],
        "A_wo": _A_in["wo"],
        "B_x": _B_in["x"],
        "B_k_cache": _B_in["k_cache"],
        "B_v_cache": _B_in["v_cache"],
        "B_wq": _B_in["wq"],
        "B_wo": _B_in["wo"],
    }

def reference(A_x, A_k_cache, A_v_cache, A_wq, A_wo, B_x, B_k_cache, B_v_cache, B_wq, B_wo):
    def _A_ref(x, k_cache, v_cache, wq, wo):
        B, Sq, D = x.shape
        Hq, Dh = 32, 128
        Hkv, Sk = k_cache.shape[1], k_cache.shape[2]
        W = min(4096, max(256, Sk // 4))
        rep = Hq // Hkv
        q = (lambda x, *d: x.permute(*d))((x @ wq).reshape(B, Sq, Hq, Dh), 0, 2, 1, 3)
        k = (lambda x, n, axis: torch.repeat_interleave(x, n, dim=axis))(k_cache[:, :, Sk - W:, :], rep, axis=1)
        v = (lambda x, n, axis: torch.repeat_interleave(x, n, dim=axis))(v_cache[:, :, Sk - W:, :], rep, axis=1)
        scores = torch.einsum("bhqd,bhkd->bhqk", q, k) * (Dh ** -0.5)
        p = _softmax(scores, axis=-1)
        o = torch.einsum("bhqk,bhkd->bhqd", p, v)
        return (lambda x, *d: x.permute(*d))(o, 0, 2, 1, 3).reshape(B, Sq, Hq * Dh) @ wo

    def _B_ref(x, k_cache, v_cache, wq, wo):
        B, Sq, D = x.shape
        Hq, Dh = 32, 128
        Hkv = k_cache.shape[1]
        rep = Hq // Hkv
        q = (lambda x, *d: x.permute(*d))((x @ wq).reshape(B, Sq, Hq, Dh), 0, 2, 1, 3)
        k = (lambda x, n, axis: torch.repeat_interleave(x, n, dim=axis))(k_cache, rep, axis=1)
        v = (lambda x, n, axis: torch.repeat_interleave(x, n, dim=axis))(v_cache, rep, axis=1)
        scores = torch.einsum("bhqd,bhkd->bhqk", q, k) * (Dh ** -0.5)
        p = _softmax(scores, axis=-1)
        o = torch.einsum("bhqk,bhkd->bhqd", p, v)
        return (lambda x, *d: x.permute(*d))(o, 0, 2, 1, 3).reshape(B, Sq, Hq * Dh) @ wo

    a = _A_ref(A_x, A_k_cache, A_v_cache, A_wq, A_wo)
    b = _B_ref(B_x, B_k_cache, B_v_cache, B_wq, B_wo)
    return torch.concatenate([a, b], axis=-1)
```

```cpp
#include <hip/hip_runtime.h>
#include <hip/hip_bf16.h>
#include <stdint.h>
typedef unsigned short u16;
typedef __attribute__((ext_vector_type(4))) unsigned u32x4;
__device__ __forceinline__ float bf2f(u16 v){ return __uint_as_float(((unsigned)v)<<16); }
__device__ __forceinline__ u16 f2bf(float f){ __hip_bfloat16 h = __float2bfloat16(f); return *(u16*)&h; }

__global__ void qk(const u16* xA,const u16* wA,const u16* xB,const u16* wB,float* q){
  int br=blockIdx.y; const u16* x=br?xB:xA; const u16* w=br?wB:wA;
  __shared__ float red[4][2][64];
  int c=blockIdx.x*64+(threadIdx.x&63); int part=threadIdx.x>>6;
  float a0=0,a1=0;
  for(int r=part*128;r<part*128+128;++r){ float wv=bf2f(w[(size_t)r*4096+c]); a0+=bf2f(x[r])*wv; a1+=bf2f(x[512+r])*wv; }
  red[part][0][threadIdx.x&63]=a0; red[part][1][threadIdx.x&63]=a1;
  __syncthreads();
  if(threadIdx.x<128){ int b=threadIdx.x>>6,l=threadIdx.x&63; float s=red[0][b][l]+red[1][b][l]+red[2][b][l]+red[3][b][l];
    q[(br*2+b)*4096+blockIdx.x*64+l]=bf2f(f2bf(s)); }
}
__global__ void attn(const u16* kA,const u16* vA,const u16* kB,const u16* vB,const float* q,float* part){
  int id=blockIdx.x;
  int br,bk,s,key0;
  if(id<256){br=0;bk=id>>4;s=id&15;key0=6144+s*128;} else {id-=256;br=1;bk=id>>6;s=id&63;key0=s*128;}
  const u16* K=(br?kB:kA)+((size_t)bk*8192+key0)*128; const u16* V=(br?vB:vA)+((size_t)bk*8192+key0)*128;
  int b=bk>>3,kvh=bk&7;
  __shared__ float qs[4][128]; __shared__ float p[4][128]; __shared__ float mx[4],sm[4];
  int t=threadIdx.x;
  for(int h=0;h<4;++h) qs[h][t]=q[(br*2+b)*4096+(kvh*4+h)*128+t];
  __syncthreads();
  float sc[4]={0,0,0,0};
  for(int d=0;d<128;d+=8){ u32x4 kv=*(const u32x4*)(K+(size_t)t*128+d);
    for(int j=0;j<4;++j){ float k0=__uint_as_float(kv[j]<<16),k1=__uint_as_float(kv[j]&0xffff0000u);
      for(int h=0;h<4;++h) sc[h]+=k0*qs[h][d+2*j]+k1*qs[h][d+2*j+1]; } }
  for(int h=0;h<4;++h) p[h][t]=sc[h]*0.08838834764831845f;
  __syncthreads();
  if(t<4){ float m=-INFINITY; for(int i=0;i<128;++i) m=fmaxf(m,p[t][i]); float s2=0; for(int i=0;i<128;++i){ float e=__expf(p[t][i]-m); p[t][i]=e; s2+=e;} mx[t]=m; sm[t]=s2; }
  __syncthreads();
  float acc[4]={0,0,0,0};
  for(int i=0;i<128;++i){ float v=bf2f(V[(size_t)i*128+t]); for(int h=0;h<4;++h) acc[h]+=p[h][i]*v; }
  float* o=part+(size_t)blockIdx.x*4*130;
  for(int h=0;h<4;++h){ o[h*130+t]=acc[h]; if(t==0){o[h*130+128]=mx[h];o[h*130+129]=sm[h];} }
}
__global__ void comb(const float* part,float* o){
  int br=blockIdx.x>>6,b=(blockIdx.x>>5)&1,h=blockIdx.x&31; int bk=b*8+(h>>2),hh=h&3; int n=br?64:16; int base=br?256+bk*64:bk*16;
  float M=-INFINITY; for(int s=0;s<n;++s) M=fmaxf(M,part[((size_t)(base+s)*4+hh)*130+128]);
  float L=0,a=0; for(int s=0;s<n;++s){ const float* r=part+((size_t)(base+s)*4+hh)*130; float w=__expf(r[128]-M); L+=w*r[129]; a+=w*r[threadIdx.x]; }
  o[(br*2+b)*4096+h*128+threadIdx.x]=bf2f(f2bf(a/L));
}
__global__ void outp(const float* o,const u16* wA,const u16* wB,u16* out){
  int br=blockIdx.y; const u16* w=br?wB:wA; __shared__ float red[16][2][64];
  int l=threadIdx.x&63,part=threadIdx.x>>6; int c=blockIdx.x*64+l; float a0=0,a1=0;
  for(int r=part*256;r<part*256+256;++r){ float wv=bf2f(w[(size_t)r*512+c]); a0+=o[(br*2)*4096+r]*wv; a1+=o[(br*2+1)*4096+r]*wv; }
  red[part][0][l]=a0; red[part][1][l]=a1; __syncthreads();
  if(threadIdx.x<128){ int b=threadIdx.x>>6; float s=0; for(int i=0;i<16;++i) s+=red[i][b][l]; out[b*1024+br*512+c]=f2bf(s); }
}
extern "C" void kernel_launch(void* const* d_in, const int* in_sizes, int n_in, void* d_out, int out_size, void* d_ws, size_t ws_size, hipStream_t stream){
  const u16* xA=(const u16*)d_in[0],*kA=(const u16*)d_in[1],*vA=(const u16*)d_in[2],*wqA=(const u16*)d_in[3],*woA=(const u16*)d_in[4];
  const u16* xB=(const u16*)d_in[5],*kB=(const u16*)d_in[6],*vB=(const u16*)d_in[7],*wqB=(const u16*)d_in[8],*woB=(const u16*)d_in[9];
  float* q=(float*)d_ws; float* o=q+16384; float* part=o+16384;
  qk<<<dim3(64,2),256,0,stream>>>(xA,wqA,xB,wqB,q);
  attn<<<1280,128,0,stream>>>(kA,vA,kB,vB,q,part);
  comb<<<128,128,0,stream>>>(part,o);
  outp<<<dim3(8,2),1024,0,stream>>>(o,woA,woB,(u16*)d_out);
}
```

```cpp
#include <hip/hip_runtime.h>
#include <hip/hip_bf16.h>
#include <stdint.h>

typedef unsigned char u8;
typedef unsigned short u16;
typedef __attribute__((ext_vector_type(4))) unsigned u32x4;
typedef __attribute__((ext_vector_type(2))) unsigned u32x2;
typedef __attribute__((ext_vector_type(4))) float f32x4;
typedef __attribute__((ext_vector_type(2))) float f32x2;
typedef __attribute__((ext_vector_type(8))) __bf16 bf16x8;
typedef __attribute__((ext_vector_type(2))) __bf16 bf16x2;
typedef __attribute__((ext_vector_type(4))) short s16x4;
typedef __amdgpu_buffer_rsrc_t rsrc_t;

#define SB __builtin_amdgcn_sched_barrier(0)
#define CLOB asm volatile("" ::: "memory")
#define WVM(n) do { SB; __builtin_amdgcn_s_waitcnt(((n) & 15) | (((n) >> 4) << 14) | 0x0f70); SB; } while (0)
#define DEVI __device__ __forceinline__

#define WS_EPOCH 0u
#define WS_Q     4096u
#define WS_ML    (WS_Q + 65536u)
#define WS_H1    (WS_ML + 32768u)
#define WS_H2    (WS_H1 + 2097152u)
#define WS_END   (WS_H2 + 2097152u)
#define SPIN_CAP 100000

#define AUX_NT  2
#define AUX_SC1 16

DEVI rsrc_t mk(const void* p, unsigned bytes) {
  return __builtin_amdgcn_make_buffer_rsrc((void*)p, (short)0, (int)bytes, 0x00020000);
}
DEVI u32x4 ld_nt(rsrc_t r, unsigned voff, unsigned soff) {
  return __builtin_amdgcn_raw_buffer_load_b128(r, (int)voff, (int)soff, AUX_NT);
}
DEVI u32x4 ld_sc1(rsrc_t r, unsigned voff, unsigned soff) {
  return __builtin_amdgcn_raw_buffer_load_b128(r, (int)voff, (int)soff, AUX_SC1);
}
DEVI u32x2 ld8_sc1(rsrc_t r, unsigned voff, unsigned soff) {
  return __builtin_amdgcn_raw_buffer_load_b64(r, (int)voff, (int)soff, AUX_SC1);
}
DEVI void st_sc1(u32x4 v, rsrc_t r, unsigned voff, unsigned soff) {
  __builtin_amdgcn_raw_buffer_store_b128(v, r, (int)voff, (int)soff, AUX_SC1);
}
DEVI float asf(unsigned v) { return __uint_as_float(v); }
DEVI unsigned asu(float v) { return __float_as_uint(v); }
DEVI float blo(unsigned v) { return asf(v << 16); }
DEVI float bhi(unsigned v) { return asf(v & 0xffff0000u); }
DEVI unsigned pk(float a, float b) {
  f32x2 f = {a, b};
  bf16x2 h = __builtin_convertvector(f, bf16x2);
  return __builtin_bit_cast(unsigned, h);
}
template <int CTRL> DEVI float dpp(float v) {
  return asf((unsigned)__builtin_amdgcn_update_dpp(0, (int)asu(v), CTRL, 0xf, 0xf, true));
}
template <int CTRL> DEVI unsigned dppu(unsigned v) {
  return (unsigned)__builtin_amdgcn_update_dpp(0, (int)v, CTRL, 0xf, 0xf, true);
}
#define QP(a, b, c, d) ((a) | ((b) << 2) | ((c) << 4) | ((d) << 6))
#define ROR(n) (0x120 + (n))
#define NBC(n) (0x150 + (n))
DEVI u32x2 sw32(float a, float b) { auto r = __builtin_amdgcn_permlane32_swap(asu(a), asu(b), false, false); return u32x2{r[0], r[1]}; }
DEVI u32x2 sw16(float a, float b) { auto r = __builtin_amdgcn_permlane16_swap(asu(a), asu(b), false, false); return u32x2{r[0], r[1]}; }
DEVI float add32(float a, float b) { u32x2 r = sw32(a, b); return asf(r[0]) + asf(r[1]); }
DEVI float add16(float a, float b) { u32x2 r = sw16(a, b); return asf(r[0]) + asf(r[1]); }
DEVI float max32(float a) { u32x2 r = sw32(a, a); return fmaxf(asf(r[0]), asf(r[1])); }
DEVI float max16(float a) { u32x2 r = sw16(a, a); return fmaxf(asf(r[0]), asf(r[1])); }
DEVI float ex2(float x) { return __builtin_amdgcn_exp2f(x); }
DEVI f32x4 mfma32(u32x4 a, u32x4 b, f32x4 c) {
  return __builtin_amdgcn_mfma_f32_16x16x32_bf16(__builtin_bit_cast(bf16x8, a), __builtin_bit_cast(bf16x8, b), c, 0, 0, 0);
}
DEVI f32x4 mfma16(unsigned a0, unsigned a1, unsigned b0, unsigned b1, f32x4 c) {
  u32x2 a = {a0, a1}, b = {b0, b1};
  return __builtin_amdgcn_mfma_f32_16x16x16bf16_1k(__builtin_bit_cast(s16x4, a), __builtin_bit_cast(s16x4, b), c, 0, 0, 0);
}
DEVI void bar() { __builtin_amdgcn_fence(__ATOMIC_RELEASE, "workgroup", "local"); __builtin_amdgcn_s_barrier(); __builtin_amdgcn_fence(__ATOMIC_ACQUIRE, "workgroup", "local"); }

#define ST(i)

DEVI float qk_group(const u32x4* k, const u32x4* qf, bool odd) {
  f32x4 d = {0.f, 0.f, 0.f, 0.f};
  d = mfma32(k[0], qf[0], d);
  d = mfma32(k[1], qf[1], d);
  d = mfma32(k[2], qf[2], d);
  d = mfma32(k[3], qf[3], d);
  float a = odd ? d[3] : d[0], b = odd ? d[1] : d[2];
  float u = a + dpp<QP(1, 0, 3, 2)>(b);
  u = blo(pk(u, u)) * 0.08838834764831845f;
  return blo(pk(u, u));
}
DEVI float head_max(float m) {
  m = fmaxf(m, dpp<QP(1, 0, 3, 2)>(m));
  m = fmaxf(m, dpp<QP(2, 3, 0, 1)>(m));
  return max32(max16(m));
}
DEVI float head_sum(float m) {
  m += dpp<QP(1, 0, 3, 2)>(m);
  m += dpp<QP(2, 3, 0, 1)>(m);
  m = add16(m, m);
  return add32(m, m);
}
template <int I> DEVI void pv_one(u32x4 v, unsigned P, unsigned m0, unsigned m1, f32x4& lo, f32x4& hi) {
  unsigned pb = dppu<QP(I, I, I, I)>(P);
  unsigned x0 = pb & m0, x1 = pb & m1;
  lo = mfma16(v[0], v[1], x0, x1, lo);
  hi = mfma16(v[2], v[3], x0, x1, hi);
}
DEVI void pv_group(const u32x4* v, unsigned P, unsigned m0, unsigned m1, f32x4& lo, f32x4& hi) {
  pv_one<0>(v[0], P, m0, m1, lo, hi);
  pv_one<1>(v[1], P, m0, m1, lo, hi);
  pv_one<2>(v[2], P, m0, m1, lo, hi);
  pv_one<3>(v[3], P, m0, m1, lo, hi);
}

struct Smem {
  f32x4 qred[8][64];
  unsigned qs[2][4][64];
  f32x4 acc[2][8][128];
  f32x2 ml[2][4][8];
  float o2[2][160];
};

__global__ void __launch_bounds__(512, 2)
fused_decode(const u8* __restrict__ xA, const u8* __restrict__ kA, const u8* __restrict__ vA, const u8* __restrict__ wqA,
             const u8* __restrict__ woA, const u8* __restrict__ xB, const u8* __restrict__ kB, const u8* __restrict__ vB,
             const u8* __restrict__ wqB, const u8* __restrict__ woB, u8* __restrict__ out, u8* ws
) {
  __shared__ Smem sm;
  ST(0);
  const int tid = threadIdx.x, l = tid & 63, w = __builtin_amdgcn_readfirstlane(tid >> 6);
  const int blk = blockIdx.x, kvh = blk & 7, j = blk >> 3, b = j >> 4, s = j & 15, g = b * 8 + kvh;
  const float LOG2E = 1.4426950408889634f;

  const rsrc_t rws = mk(ws, WS_END);
  const rsrc_t rx = mk(b ? xB : xA, 2048);
  const rsrc_t rwq = mk(b ? wqB : wqA, 4194304);
  const rsrc_t rwo = mk((b ? woB : woA) + (size_t)(kvh * 512 + s * 32) * 1024, 32768);
  const rsrc_t rkA = mk(kA + ((size_t)g * 8192 + 6144) * 256, 524288);
  const rsrc_t rvA = mk(vA + ((size_t)g * 8192 + 6144) * 256, 524288);
  const rsrc_t rkB = mk(kB + (size_t)g * 8192 * 256, 2097152);
  const rsrc_t rvB = mk(vB + (size_t)g * 8192 * 256, 2097152);

  const unsigned ep = __builtin_amdgcn_raw_buffer_load_b32(rws, 0, WS_EPOCH, AUX_SC1);
  const u32x2 x0 = __builtin_amdgcn_raw_buffer_load_b64(rx, (l >> 2) * 8, w * 128, 0);
  const u32x2 x1 = __builtin_amdgcn_raw_buffer_load_b64(rx, (l >> 2) * 8 + 1024, w * 128, 0);
  SB;
  u32x4 wq[4], ka[4], va[4], kb[16], vb[16], wo[4];
  {
    const unsigned vo = (l >> 2) * 32768 + (l & 3) * 16, so = w * 524288 + kvh * 1024 + s * 64;
#pragma unroll
    for (int i = 0; i < 4; ++i) { wq[i] = ld_nt(rwq, vo, so + i * 8192); SB; }
  }
  const unsigned vok = (4 * ((l >> 1) & 1) + ((l >> 2) & 3)) * 256 + (l >> 4) * 32 + (l & 1) * 16, vov = l * 16;
#define SOA(t) ((unsigned)((s * 16 + w * 2 + (t) + 17 * g) & 255) * 2048u)
#define SOB(t) ((unsigned)((s * 64 + w * 8 + (t) + 65 * g) & 1023) * 2048u)
#pragma unroll
  for (int i = 0; i < 4; ++i) { ka[i] = ld_nt(rkA, vok + (i & 1) * 128, SOA(i >> 1)); SB; }
#define LDK(a, e) _Pragma("unroll") for (int i = a; i < e; ++i) { kb[i] = ld_nt(rkB, vok + (i & 1) * 128, SOB(i >> 1)); SB; }
#define LDV(a, e) _Pragma("unroll") for (int i = a; i < e; ++i) { vb[i] = ld_nt(rvB, vov + (i & 1) * 1024, SOB(i >> 1)); SB; }

  {
    float a[16];
#pragma unroll
    for (int i = 0; i < 16; ++i) a[i] = 0.f;
#pragma unroll
    for (int i = 0; i < 4; ++i) {
      const float xa = (i & 1) ? bhi(x0[i >> 1]) : blo(x0[i >> 1]), xb = (i & 1) ? bhi(x1[i >> 1]) : blo(x1[i >> 1]);
#pragma unroll
      for (int c = 0; c < 4; ++c) {
        const float e0 = blo(wq[i][c]), e1 = bhi(wq[i][c]);
        a[2 * c] = fmaf(e0, xa, a[2 * c]);
        a[2 * c + 1] = fmaf(e1, xa, a[2 * c + 1]);
        a[8 + 2 * c] = fmaf(e0, xb, a[8 + 2 * c]);
        a[8 + 2 * c + 1] = fmaf(e1, xb, a[8 + 2 * c + 1]);
      }
    }
#pragma unroll
    for (int i = 0; i < 8; ++i) a[i] = add32(a[i], a[i + 8]);
#pragma unroll
    for (int i = 0; i < 4; ++i) a[i] = add16(a[i], a[i + 4]);
    sm.qred[w][l] = f32x4{a[0], a[1], a[2], a[3]};
  }
  const unsigned tag = __builtin_amdgcn_readfirstlane(ep) + 1u;
  ST(1);
  bar();
  if (w == 0) {
    f32x4 t = sm.qred[0][l];
#pragma unroll
    for (int i = 1; i < 8; ++i) t += sm.qred[i][l];
#pragma unroll
    for (int i = 0; i < 4; ++i) { t[i] += dpp<ROR(4)>(t[i]); t[i] += dpp<ROR(8)>(t[i]); }
    if ((l & 12) == 0) {
      u32x4 gq = {pk(t[0], t[1]), tag, pk(t[2], t[3]), tag};
      st_sc1(gq, rws, (l >> 5) * 16384 + (l & 3) * 32 + ((l >> 4) & 1) * 16, WS_Q + b * 32768 + kvh * 2048 + s * 128);
    }
  }
  SB;
  ST(2);
#pragma unroll
  for (int i = 0; i < 4; ++i) { va[i] = ld_nt(rvA, vov + (i & 1) * 1024, SOA(i >> 1)); SB; }
  LDK(0, 4)
  WVM(8);
  LDK(4, 8)
  const unsigned soq = WS_Q + (w >> 2) * 32768 + b * 16384 + kvh * 2048 + (w & 3) * 512;
  u32x2 qg = ld8_sc1(rws, l * 8, soq);
  WVM(9);
  LDK(8, 12)
  WVM(9);
  LDK(12, 16)
  if (!__all(qg[1] == tag)) {
    for (int it = 0; it < SPIN_CAP; ++it) {
      CLOB;
      qg = ld8_sc1(rws, l * 8, soq);
      if (__all(qg[1] == tag)) break;
    }
  }
  sm.qs[w >> 2][w & 3][l] = qg[0];
  SB;
  LDV(0, 4)
  ST(3);
  bar();
  ST(4);

  const bool odd = l & 1;
  const int h = (l >> 2) & 3;
  const unsigned m0 = (l & 2) ? 0u : (odd ? 0xffff0000u : 0xffffu), m1 = (l & 2) ? (odd ? 0xffff0000u : 0xffffu) : 0u;
  const unsigned t0 = (l & 2) ? 0u : ~0u;
  u32x4 qf[4];
  {
    const u32x4 q0 = *(const u32x4*)&sm.qs[0][h][(l >> 4) * 8 + (l & 1) * 4], q1 = *(const u32x4*)&sm.qs[0][h][32 + (l >> 4) * 8 + (l & 1) * 4];
    qf[0] = q0 & t0; qf[1] = q1 & t0; qf[2] = q0 & ~t0; qf[3] = q1 & ~t0;
    const float u = qk_group(ka, qf, odd);
    const float m2 = head_max(u) * LOG2E;
    const float p = ex2(fmaf(u, LOG2E, -m2));
    const float ls = head_sum(p);
    f32x4 lo = {0.f, 0.f, 0.f, 0.f}, hi = lo;
    pv_group(va, pk(p, p), m0, m1, lo, hi);
    sm.acc[0][w][2 * l] = lo; sm.acc[0][w][2 * l + 1] = hi;
    if ((l & 0x33) == 0) sm.ml[0][h][w] = f32x2{m2, ls};
  }
  SB;
  ST(5);
  unsigned P[4];
  float m2B;
  {
    const u32x4 q0 = *(const u32x4*)&sm.qs[1][h][(l >> 4) * 8 + (l & 1) * 4], q1 = *(const u32x4*)&sm.qs[1][h][32 + (l >> 4) * 8 + (l & 1) * 4];
    qf[0] = q0 & t0; qf[1] = q1 & t0; qf[2] = q0 & ~t0; qf[3] = q1 & ~t0;
    float u[4];
    u[0] = qk_group(kb, qf, odd);
    u[1] = qk_group(kb + 4, qf, odd);
    SB;
    LDV(4, 8)
    u[2] = qk_group(kb + 8, qf, odd);
    SB;
    LDV(8, 12)
    u[3] = qk_group(kb + 12, qf, odd);
    SB;
    LDV(12, 16)
    m2B = head_max(fmaxf(fmaxf(u[0], u[1]), fmaxf(u[2], u[3]))) * LOG2E;
    float ls = 0.f;
#pragma unroll
    for (int i = 0; i < 4; ++i) { const float p = ex2(fmaf(u[i], LOG2E, -m2B)); ls += p; P[i] = pk(p, p); }
    ls = head_sum(ls);
    if ((l & 0x33) == 0) sm.ml[1][h][w] = f32x2{m2B, ls};
  }
  ST(6);
  bar();
  ST(7);
  float fB;
  {
    const f32x4* mp = (const f32x4*)&sm.ml[1][h][0];
    const f32x4 a0 = mp[0], a1 = mp[1], a2 = mp[2], a3 = mp[3];
    const float M = fmaxf(fmaxf(fmaxf(a0[0], a0[2]), fmaxf(a1[0], a1[2])), fmaxf(fmaxf(a2[0], a2[2]), fmaxf(a3[0], a3[2])));
    fB = ex2(m2B - M);
  }
  if (w < 4) {
    const int hh = (l >> 4) & 3;
    float mw[8], M = -INFINITY;
#pragma unroll
    for (int i = 0; i < 8; ++i) { mw[i] = sm.ml[0][hh][i][0]; M = fmaxf(M, mw[i]); }
    f32x2 t = {0.f, 0.f};
#pragma unroll
    for (int i = 0; i < 8; ++i) t += ((const f32x2*)&sm.acc[0][i][0])[tid] * ex2(mw[i] - M);
    st_sc1(u32x4{asu(t[0]), tag, asu(t[1]), tag}, rws, hh * 32768 + (l & 15) * 16, WS_H1 + kvh * 131072 + w * 8192 + j * 256);
  } else if (w == 4) {
    const f32x2 v = sm.ml[(l >> 5) & 1][(l >> 3) & 3][l & 7];
    float M = v[0];
    M = fmaxf(M, dpp<QP(1, 0, 3, 2)>(M));
    M = fmaxf(M, dpp<QP(2, 3, 0, 1)>(M));
    M = fmaxf(M, dpp<0x141>(M));
    float L = v[1] * ex2(v[0] - M);
    L += dpp<QP(1, 0, 3, 2)>(L);
    L += dpp<QP(2, 3, 0, 1)>(L);
    L += dpp<0x141>(L);
    if ((l & 7) == 0) st_sc1(u32x4{asu(M), tag, asu(L), tag}, rws, (l >> 5) * 16384 + ((l >> 3) & 3) * 512, WS_ML + kvh * 2048 + j * 16);
  }
  SB;
  ST(8);
  {
    f32x4 lo = {0.f, 0.f, 0.f, 0.f}, hi = lo;
    pv_group(vb, P[0], m0, m1, lo, hi);
    WVM(12);
    {
      const unsigned vo = (l >> 3) * 1024 + (l & 7) * 16;
#pragma unroll
      for (int i = 0; i < 4; ++i) { wo[i] = ld_nt(rwo, vo, ((i + s) & 3) * 8192 + w * 128); SB; }
    }
#pragma unroll
    for (int G = 1; G < 4; ++G) pv_group(vb + 4 * G, P[G], m0, m1, lo, hi);
    sm.acc[1][w][2 * l] = lo * fB; sm.acc[1][w][2 * l + 1] = hi * fB;
  }
  ST(9);
  bar();
  if (w < 4) {
    f32x2 t[8];
#pragma unroll
    for (int i = 0; i < 8; ++i) t[i] = ((const f32x2*)&sm.acc[1][i][0])[tid];
    const f32x2 r = ((t[0] + t[1]) + (t[2] + t[3])) + ((t[4] + t[5]) + (t[6] + t[7]));
    st_sc1(u32x4{asu(r[0]), tag, asu(r[1]), tag}, rws, ((l >> 4) & 3) * 32768 + (l & 15) * 16, WS_H1 + 1048576 + kvh * 131072 + w * 8192 + j * 256);
  }
  SB;
  ST(10);
  {
    const unsigned soml = WS_ML + b * 16384 + kvh * 2048 + (s >> 2) * 512 + (w >> 2) * 256;
    const unsigned voml = (((l & 15) + (l >> 4) + 4 * (w & 3)) & 15) * 16;
    const unsigned soh = WS_H1 + b * 1048576 + kvh * 131072 + s * 8192 + w * 1024;
    u32x4 gm = ld_sc1(rws, voml, soml);
    u32x4 gd = ld_sc1(rws, l * 16, soh);
    SB;
    if (!__all(gm[1] == tag && gm[3] == tag)) {
      for (int it = 0; it < SPIN_CAP; ++it) {
      CLOB;
        gm = ld_sc1(rws, voml, soml);
        if (__all(gm[1] == tag && gm[3] == tag)) break;
      }
    }
    const float mm = asf(gm[0]);
    float M = fmaxf(mm, dpp<ROR(1)>(mm));
    M = fmaxf(M, dpp<ROR(2)>(M));
    M = fmaxf(M, dpp<ROR(4)>(M));
    M = fmaxf(M, dpp<ROR(8)>(M));
    const float e = ex2(mm - M);
    float L = e * asf(gm[2]);
    L += dpp<ROR(1)>(L);
    L += dpp<ROR(2)>(L);
    L += dpp<ROR(4)>(L);
    L += dpp<ROR(8)>(L);
    const float wgt = dpp<NBC(0)>(e / L);
    SB;
    if (!__all(gd[1] == tag && gd[3] == tag)) {
      for (int it = 0; it < SPIN_CAP; ++it) {
      CLOB;
        gd = ld_sc1(rws, l * 16, soh);
        if (__all(gd[1] == tag && gd[3] == tag)) break;
      }
    }
    float v = add32(asf(gd[0]) * wgt, asf(gd[2]) * wgt);
    v = add16(v, v);
    const int row = 16 * (l & 1) + 8 * (l >> 5) + 4 * ((l >> 1) & 1) + ((l >> 2) & 3);
    sm.o2[w >> 2][row * 4 + (w & 3)] = v;
  }
  ST(11);
  bar();
  {
    f32x4 lo = {0.f, 0.f, 0.f, 0.f}, hi = lo;
#pragma unroll
    for (int i = 0; i < 4; ++i) {
      const f32x4 t = *(const f32x4*)&sm.o2[(l >> 2) & 1][(((i + s) & 3) * 8 + (l >> 4) * 2 + ((l >> 3) & 1)) * 4];
      const float o = (t[0] + t[1]) + (t[2] + t[3]);
      const unsigned pb = pk(o, o);
      const unsigned x0_ = pb & m0, x1_ = pb & m1;
      lo = mfma16(wo[i][0], wo[i][1], x0_, x1_, lo);
      hi = mfma16(wo[i][2], wo[i][3], x0_, x1_, hi);
    }
    float r[4];
#pragma unroll
    for (int i = 0; i < 4; ++i) {
      const u32x2 t = sw32(lo[i], hi[i]);
      r[i] = i < 2 ? asf(t[0]) + dpp<ROR(8)>(asf(t[1])) : asf(t[1]) + dpp<ROR(8)>(asf(t[0]));
    }
    const bool up = l & 8;
    st_sc1(u32x4{asu(up ? r[2] : r[0]), tag, asu(up ? r[3] : r[1]), tag}, rws, (l >> 2) * 8192 + (l & 3) * 16,
           WS_H2 + b * 1048576 + w * 131072 + (kvh * 16 + s) * 64);
  }
  SB;
  ST(12);
  if (w == 0) {
    const int ci = kvh * 16 + s;
    const unsigned so = WS_H2 + b * 1048576 + ci * 8192;
    u32x4 gg[8];
    for (int it = 0; it < SPIN_CAP; ++it) {
      CLOB;
#pragma unroll
      for (int i = 0; i < 8; ++i) gg[i] = ld_sc1(rws, l * 16 + (i & 3) * 1024, so + (i >> 2) * 4096);
      bool ok = true;
#pragma unroll
      for (int i = 0; i < 8; ++i) ok = ok && gg[i][1] == tag && gg[i][3] == tag;
      if (__all(ok)) break;
    }
    const float v0 = ((asf(gg[0][0]) + asf(gg[1][0])) + (asf(gg[2][0]) + asf(gg[3][0]))) + ((asf(gg[4][0]) + asf(gg[5][0])) + (asf(gg[6][0]) + asf(gg[7][0])));
    const float v1 = ((asf(gg[0][2]) + asf(gg[1][2])) + (asf(gg[2][2]) + asf(gg[3][2]))) + ((asf(gg[4][2]) + asf(gg[5][2])) + (asf(gg[6][2]) + asf(gg[7][2])));
    float v = add32(v0, v1);
    v = add16(v, v);
    v += dpp<ROR(8)>(v);
    v += dpp<ROR(4)>(v);
    if ((l & 0x1c) == 0) {
      const int col = 64 * (ci >> 4) + 32 * ((ci >> 2) & 1) + 16 * ((ci >> 1) & 1) + 8 * (l >> 5) + 4 * ((ci >> 3) & 1) + (l & 3);
      ((u16*)out)[(ci & 1) * 1024 + b * 512 + col] = (u16)pk(v, v);
      if (blk == 0 && l == 0) __builtin_amdgcn_raw_buffer_store_b32(tag, rws, 0, WS_EPOCH, AUX_SC1);
    }
  }
}

extern "C" void kernel_launch(void* const* d_in, const int* in_sizes, int n_in, void* d_out, int out_size, void* d_ws,
                              size_t ws_size, hipStream_t stream) {
  fused_decode<<<256, 512, 0, stream>>>((const u8*)d_in[0], (const u8*)d_in[1], (const u8*)d_in[2], (const u8*)d_in[3],
                                        (const u8*)d_in[4], (const u8*)d_in[5], (const u8*)d_in[6], (const u8*)d_in[7],
                                        (const u8*)d_in[8], (const u8*)d_in[9], (u8*)d_out, (u8*)d_ws);
}
```

```cpp
#define DLY_I 0
#define DLY_J 0
#define DLY_K 0
#define DLY_B 0
#include <hip/hip_runtime.h>
#include <hip/hip_bf16.h>
#include <stdint.h>

typedef unsigned char u8;
typedef unsigned short u16;
typedef __attribute__((ext_vector_type(4))) unsigned u32x4;
typedef __attribute__((ext_vector_type(2))) unsigned u32x2;
typedef __attribute__((ext_vector_type(4))) float f32x4;
typedef __attribute__((ext_vector_type(2))) float f32x2;
typedef __attribute__((ext_vector_type(8))) __bf16 bf16x8;
typedef __attribute__((ext_vector_type(2))) __bf16 bf16x2;
typedef __attribute__((ext_vector_type(4))) short s16x4;
typedef __amdgpu_buffer_rsrc_t rsrc_t;

#define SB __builtin_amdgcn_sched_barrier(0)
#define CLOB asm volatile("" ::: "memory")
#define WVM(n) do { SB; __builtin_amdgcn_s_waitcnt(((n) & 15) | (((n) >> 4) << 14) | 0x0f70); SB; } while (0)
#define DEVI __device__ __forceinline__

#define WS_EPOCH 0u
#define WS_Q     4096u
#define WS_ML    (WS_Q + 65536u)
#define WS_H1    (WS_ML + 32768u)
#define WS_H2    (WS_H1 + 2097152u)
#define WS_END   (WS_H2 + 2097152u)
#define SPIN_CAP 100000

#define AUX_NT  2
#define AUX_SC1 16

DEVI rsrc_t mk(const void* p, unsigned bytes) {
  return __builtin_amdgcn_make_buffer_rsrc((void*)p, (short)0, (int)bytes, 0x00020000);
}
DEVI u32x4 ld_nt(rsrc_t r, unsigned voff, unsigned soff) {
  return __builtin_amdgcn_raw_buffer_load_b128(r, (int)voff, (int)soff, AUX_NT);
}
DEVI u32x4 ld_sc1(rsrc_t r, unsigned voff, unsigned soff) {
  return __builtin_amdgcn_raw_buffer_load_b128(r, (int)voff, (int)soff, AUX_SC1);
}
DEVI u32x2 ld8_sc1(rsrc_t r, unsigned voff, unsigned soff) {
  return __builtin_amdgcn_raw_buffer_load_b64(r, (int)voff, (int)soff, AUX_SC1);
}
DEVI void st_sc1(u32x4 v, rsrc_t r, unsigned voff, unsigned soff) {
  __builtin_amdgcn_raw_buffer_store_b128(v, r, (int)voff, (int)soff, AUX_SC1);
}
DEVI float asf(unsigned v) { return __uint_as_float(v); }
DEVI unsigned asu(float v) { return __float_as_uint(v); }
DEVI float blo(unsigned v) { return asf(v << 16); }
DEVI float bhi(unsigned v) { return asf(v & 0xffff0000u); }
DEVI unsigned pk(float a, float b) {
  f32x2 f = {a, b};
  bf16x2 h = __builtin_convertvector(f, bf16x2);
  return __builtin_bit_cast(unsigned, h);
}
template <int CTRL> DEVI float dpp(float v) {
  return asf((unsigned)__builtin_amdgcn_update_dpp(0, (int)asu(v), CTRL, 0xf, 0xf, true));
}
template <int CTRL> DEVI unsigned dppu(unsigned v) {
  return (unsigned)__builtin_amdgcn_update_dpp(0, (int)v, CTRL, 0xf, 0xf, true);
}
#define QP(a, b, c, d) ((a) | ((b) << 2) | ((c) << 4) | ((d) << 6))
#define ROR(n) (0x120 + (n))
#define NBC(n) (0x150 + (n))
DEVI u32x2 sw32(float a, float b) { auto r = __builtin_amdgcn_permlane32_swap(asu(a), asu(b), false, false); return u32x2{r[0], r[1]}; }
DEVI u32x2 sw16(float a, float b) { auto r = __builtin_amdgcn_permlane16_swap(asu(a), asu(b), false, false); return u32x2{r[0], r[1]}; }
DEVI float add32(float a, float b) { u32x2 r = sw32(a, b); return asf(r[0]) + asf(r[1]); }
DEVI float add16(float a, float b) { u32x2 r = sw16(a, b); return asf(r[0]) + asf(r[1]); }
DEVI float max32(float a) { u32x2 r = sw32(a, a); return fmaxf(asf(r[0]), asf(r[1])); }
DEVI float max16(float a) { u32x2 r = sw16(a, a); return fmaxf(asf(r[0]), asf(r[1])); }
DEVI float ex2(float x) { return __builtin_amdgcn_exp2f(x); }
DEVI f32x4 mfma32(u32x4 a, u32x4 b, f32x4 c) {
  return __builtin_amdgcn_mfma_f32_16x16x32_bf16(__builtin_bit_cast(bf16x8, a), __builtin_bit_cast(bf16x8, b), c, 0, 0, 0);
}
DEVI f32x4 mfma16(unsigned a0, unsigned a1, unsigned b0, unsigned b1, f32x4 c) {
  u32x2 a = {a0, a1}, b = {b0, b1};
  return __builtin_amdgcn_mfma_f32_16x16x16bf16_1k(__builtin_bit_cast(s16x4, a), __builtin_bit_cast(s16x4, b), c, 0, 0, 0);
}
DEVI void bar() { __builtin_amdgcn_fence(__ATOMIC_RELEASE, "workgroup", "local"); __builtin_amdgcn_s_barrier(); __builtin_amdgcn_fence(__ATOMIC_ACQUIRE, "workgroup", "local"); }

DEVI unsigned long long rt() { unsigned long long t; asm volatile("s_memrealtime %0\n\ts_waitcnt lgkmcnt(0)" : "=s"(t)::"memory"); return t; }
#define ST(i) do { SB; stm[i] = rt(); SB; } while (0)

DEVI float qk_group(const u32x4* k, const u32x4* qf, bool odd) {
  f32x4 d = {0.f, 0.f, 0.f, 0.f};
  d = mfma32(k[0], qf[0], d);
  d = mfma32(k[1], qf[1], d);
  d = mfma32(k[2], qf[2], d);
  d = mfma32(k[3], qf[3], d);
  float a = odd ? d[3] : d[0], b = odd ? d[1] : d[2];
  float u = a + dpp<QP(1, 0, 3, 2)>(b);
  u = blo(pk(u, u)) * 0.08838834764831845f;
  return blo(pk(u, u));
}
DEVI float head_max(float m) {
  m = fmaxf(m, dpp<QP(1, 0, 3, 2)>(m));
  m = fmaxf(m, dpp<QP(2, 3, 0, 1)>(m));
  return max32(max16(m));
}
DEVI float head_sum(float m) {
  m += dpp<QP(1, 0, 3, 2)>(m);
  m += dpp<QP(2, 3, 0, 1)>(m);
  m = add16(m, m);
  return add32(m, m);
}
template <int I> DEVI void pv_one(u32x4 v, unsigned P, unsigned m0, unsigned m1, f32x4& lo, f32x4& hi) {
  unsigned pb = dppu<QP(I, I, I, I)>(P);
  unsigned x0 = pb & m0, x1 = pb & m1;
  lo = mfma16(v[0], v[1], x0, x1, lo);
  hi = mfma16(v[2], v[3], x0, x1, hi);
}
DEVI void pv_group(const u32x4* v, unsigned P, unsigned m0, unsigned m1, f32x4& lo, f32x4& hi) {
  pv_one<0>(v[0], P, m0, m1, lo, hi);
  pv_one<1>(v[1], P, m0, m1, lo, hi);
  pv_one<2>(v[2], P, m0, m1, lo, hi);
  pv_one<3>(v[3], P, m0, m1, lo, hi);
}

struct Smem {
  f32x4 qred[8][64];
  unsigned qs[2][4][64];
  f32x4 acc[2][8][128];
  f32x2 ml[2][4][8];
  float o2[2][160];
};

__global__ void __launch_bounds__(512, 2)
fused_decode(const u8* __restrict__ xA, const u8* __restrict__ kA, const u8* __restrict__ vA, const u8* __restrict__ wqA,
             const u8* __restrict__ woA, const u8* __restrict__ xB, const u8* __restrict__ kB, const u8* __restrict__ vB,
             const u8* __restrict__ wqB, const u8* __restrict__ woB, u8* __restrict__ out, u8* ws
) {
  __shared__ Smem sm;
  unsigned long long stm[16];
  ST(0);
  const int tid = threadIdx.x, l = tid & 63, w = __builtin_amdgcn_readfirstlane(tid >> 6);
  const int blk = blockIdx.x, kvh = blk & 7, j = blk >> 3, b = j >> 4, s = j & 15, g = b * 8 + kvh;
  const float LOG2E = 1.4426950408889634f;

  const rsrc_t rws = mk(ws, WS_END);
  const rsrc_t rx = mk(b ? xB : xA, 2048);
  const rsrc_t rwq = mk(b ? wqB : wqA, 4194304);
  const rsrc_t rwo = mk((b ? woB : woA) + (size_t)(kvh * 512 + s * 32) * 1024, 32768);
  const rsrc_t rkA = mk(kA + ((size_t)g * 8192 + 6144) * 256, 524288);
  const rsrc_t rvA = mk(vA + ((size_t)g * 8192 + 6144) * 256, 524288);
  const rsrc_t rkB = mk(kB + (size_t)g * 8192 * 256, 2097152);
  const rsrc_t rvB = mk(vB + (size_t)g * 8192 * 256, 2097152);

  const unsigned ep = __builtin_amdgcn_raw_buffer_load_b32(rws, 0, WS_EPOCH, AUX_SC1);
  const u32x2 x0 = __builtin_amdgcn_raw_buffer_load_b64(rx, (l >> 2) * 8, w * 128, 0);
  const u32x2 x1 = __builtin_amdgcn_raw_buffer_load_b64(rx, (l >> 2) * 8 + 1024, w * 128, 0);
  SB;
  u32x4 wq[4], ka[4], va[4], kb[16], vb[16], wo[4];
  {
    const unsigned vo = (l >> 2) * 32768 + (l & 3) * 16, so = w * 524288 + kvh * 1024 + s * 64;
#pragma unroll
    for (int i = 0; i < 4; ++i) { wq[i] = ld_nt(rwq, vo, so + i * 8192); SB; }
  }
  const unsigned vok = (4 * ((l >> 1) & 1) + ((l >> 2) & 3)) * 256 + (l >> 4) * 32 + (l & 1) * 16, vov = l * 16;
#define SOA(t) ((unsigned)((s * 16 + w * 2 + (t) + 17 * g) & 255) * 2048u)
#define SOB(t) ((unsigned)((s * 64 + w * 8 + (t) + 65 * g) & 1023) * 2048u)
#pragma unroll
  for (int i = 0; i < 4; ++i) { ka[i] = ld_nt(rkA, vok + (i & 1) * 128, SOA(i >> 1)); SB; }
#define LDK(a, e) _Pragma("unroll") for (int i = a; i < e; ++i) { kb[i] = ld_nt(rkB, vok + (i & 1) * 128, SOB(i >> 1)); SB; }
#define LDV(a, e) _Pragma("unroll") for (int i = a; i < e; ++i) { vb[i] = ld_nt(rvB, vov + (i & 1) * 1024, SOB(i >> 1)); SB; }

  {
    float a[16];
#pragma unroll
    for (int i = 0; i < 16; ++i) a[i] = 0.f;
#pragma unroll
    for (int i = 0; i < 4; ++i) {
      const float xa = (i & 1) ? bhi(x0[i >> 1]) : blo(x0[i >> 1]), xb = (i & 1) ? bhi(x1[i >> 1]) : blo(x1[i >> 1]);
#pragma unroll
      for (int c = 0; c < 4; ++c) {
        const float e0 = blo(wq[i][c]), e1 = bhi(wq[i][c]);
        a[2 * c] = fmaf(e0, xa, a[2 * c]);
        a[2 * c + 1] = fmaf(e1, xa, a[2 * c + 1]);
        a[8 + 2 * c] = fmaf(e0, xb, a[8 + 2 * c]);
        a[8 + 2 * c + 1] = fmaf(e1, xb, a[8 + 2 * c + 1]);
      }
    }
#pragma unroll
    for (int i = 0; i < 8; ++i) a[i] = add32(a[i], a[i + 8]);
#pragma unroll
    for (int i = 0; i < 4; ++i) a[i] = add16(a[i], a[i + 4]);
    sm.qred[w][l] = f32x4{a[0], a[1], a[2], a[3]};
  }
  const unsigned tag = __builtin_amdgcn_readfirstlane(ep) + 1u;
  ST(1);
  bar();
  if (w == 0) {
    f32x4 t = sm.qred[0][l];
#pragma unroll
    for (int i = 1; i < 8; ++i) t += sm.qred[i][l];
#pragma unroll
    for (int i = 0; i < 4; ++i) { t[i] += dpp<ROR(4)>(t[i]); t[i] += dpp<ROR(8)>(t[i]); }
    if ((l & 12) == 0) {
      u32x4 gq = {pk(t[0], t[1]), tag, pk(t[2], t[3]), tag};
      st_sc1(gq, rws, (l >> 5) * 16384 + (l & 3) * 32 + ((l >> 4) & 1) * 16, WS_Q + b * 32768 + kvh * 2048 + s * 128);
    }
  }
  SB;
  ST(2);
#pragma unroll
  for (int i = 0; i < 4; ++i) { va[i] = ld_nt(rvA, vov + (i & 1) * 1024, SOA(i >> 1)); SB; }
  LDK(0, 4)
  WVM(8);
  LDK(4, 8)
  const unsigned soq = WS_Q + (w >> 2) * 32768 + b * 16384 + kvh * 2048 + (w & 3) * 512;
  u32x2 qg = ld8_sc1(rws, l * 8, soq);
  WVM(9);
  LDK(8, 12)
  WVM(9);
  LDK(12, 16)
  if (!__all(qg[1] == tag)) {
    for (int it = 0; it < SPIN_CAP; ++it) {
      CLOB;
      qg = ld8_sc1(rws, l * 8, soq);
      if (__all(qg[1] == tag)) break;
    }
  }
  sm.qs[w >> 2][w & 3][l] = qg[0];
  SB;
  LDV(0, 4)
  ST(3);
  bar();
  ST(4);

  const bool odd = l & 1;
  const int h = (l >> 2) & 3;
  const unsigned m0 = (l & 2) ? 0u : (odd ? 0xffff0000u : 0xffffu), m1 = (l & 2) ? (odd ? 0xffff0000u : 0xffffu) : 0u;
  const unsigned t0 = (l & 2) ? 0u : ~0u;
  u32x4 qf[4];
  {
    const u32x4 q0 = *(const u32x4*)&sm.qs[0][h][(l >> 4) * 8 + (l & 1) * 4], q1 = *(const u32x4*)&sm.qs[0][h][32 + (l >> 4) * 8 + (l & 1) * 4];
    qf[0] = q0 & t0; qf[1] = q1 & t0; qf[2] = q0 & ~t0; qf[3] = q1 & ~t0;
    const float u = qk_group(ka, qf, odd);
    const float m2 = head_max(u) * LOG2E;
    const float p = ex2(fmaf(u, LOG2E, -m2));
    const float ls = head_sum(p);
    f32x4 lo = {0.f, 0.f, 0.f, 0.f}, hi = lo;
    pv_group(va, pk(p, p), m0, m1, lo, hi);
    sm.acc[0][w][2 * l] = lo; sm.acc[0][w][2 * l + 1] = hi;
    if ((l & 0x33) == 0) sm.ml[0][h][w] = f32x2{m2, ls};
  }
  SB;
  ST(5);
  unsigned P[4];
  float m2B;
  {
    const u32x4 q0 = *(const u32x4*)&sm.qs[1][h][(l >> 4) * 8 + (l & 1) * 4], q1 = *(const u32x4*)&sm.qs[1][h][32 + (l >> 4) * 8 + (l & 1) * 4];
    qf[0] = q0 & t0; qf[1] = q1 & t0; qf[2] = q0 & ~t0; qf[3] = q1 & ~t0;
    float u[4];
    u[0] = qk_group(kb, qf, odd);
    u[1] = qk_group(kb + 4, qf, odd);
    SB;
    LDV(4, 8)
    u[2] = qk_group(kb + 8, qf, odd);
    SB;
    LDV(8, 12)
    u[3] = qk_group(kb + 12, qf, odd);
    SB;
    LDV(12, 16)
    m2B = head_max(fmaxf(fmaxf(u[0], u[1]), fmaxf(u[2], u[3]))) * LOG2E;
    float ls = 0.f;
#pragma unroll
    for (int i = 0; i < 4; ++i) { const float p = ex2(fmaf(u[i], LOG2E, -m2B)); ls += p; P[i] = pk(p, p); }
    ls = head_sum(ls);
    if ((l & 0x33) == 0) sm.ml[1][h][w] = f32x2{m2B, ls};
  }
  ST(6);
  bar();
  ST(7);
  float fB;
  {
    const f32x4* mp = (const f32x4*)&sm.ml[1][h][0];
    const f32x4 a0 = mp[0], a1 = mp[1], a2 = mp[2], a3 = mp[3];
    const float M = fmaxf(fmaxf(fmaxf(a0[0], a0[2]), fmaxf(a1[0], a1[2])), fmaxf(fmaxf(a2[0], a2[2]), fmaxf(a3[0], a3[2])));
    fB = ex2(m2B - M);
  }
  if (w < 4) {
    const int hh = (l >> 4) & 3;
    float mw[8], M = -INFINITY;
#pragma unroll
    for (int i = 0; i < 8; ++i) { mw[i] = sm.ml[0][hh][i][0]; M = fmaxf(M, mw[i]); }
    f32x2 t = {0.f, 0.f};
#pragma unroll
    for (int i = 0; i < 8; ++i) t += ((const f32x2*)&sm.acc[0][i][0])[tid] * ex2(mw[i] - M);
    st_sc1(u32x4{asu(t[0]), tag, asu(t[1]), tag}, rws, hh * 32768 + (l & 15) * 16, WS_H1 + kvh * 131072 + w * 8192 + j * 256);
  } else if (w == 4) {
    const f32x2 v = sm.ml[(l >> 5) & 1][(l >> 3) & 3][l & 7];
    float M = v[0];
    M = fmaxf(M, dpp<QP(1, 0, 3, 2)>(M));
    M = fmaxf(M, dpp<QP(2, 3, 0, 1)>(M));
    M = fmaxf(M, dpp<0x141>(M));
    float L = v[1] * ex2(v[0] - M);
    L += dpp<QP(1, 0, 3, 2)>(L);
    L += dpp<QP(2, 3, 0, 1)>(L);
    L += dpp<0x141>(L);
    if ((l & 7) == 0) st_sc1(u32x4{asu(M), tag, asu(L), tag}, rws, (l >> 5) * 16384 + ((l >> 3) & 3) * 512, WS_ML + kvh * 2048 + j * 16);
  }
  SB;
  ST(8);
  {
    f32x4 lo = {0.f, 0.f, 0.f, 0.f}, hi = lo;
    pv_group(vb, P[0], m0, m1, lo, hi);
    WVM(12);
    {
      const unsigned vo = (l >> 3) * 1024 + (l & 7) * 16;
#pragma unroll
      for (int i = 0; i < 4; ++i) { wo[i] = ld_nt(rwo, vo, ((i + s) & 3) * 8192 + w * 128); SB; }
    }
#pragma unroll
    for (int G = 1; G < 4; ++G) pv_group(vb + 4 * G, P[G], m0, m1, lo, hi);
    sm.acc[1][w][2 * l] = lo * fB; sm.acc[1][w][2 * l + 1] = hi * fB;
  }
  ST(9);
  bar();
  if (w < 4) {
    f32x2 t[8];
#pragma unroll
    for (int i = 0; i < 8; ++i) t[i] = ((const f32x2*)&sm.acc[1][i][0])[tid];
    const f32x2 r = ((t[0] + t[1]) + (t[2] + t[3])) + ((t[4] + t[5]) + (t[6] + t[7]));
    st_sc1(u32x4{asu(r[0]), tag, asu(r[1]), tag}, rws, ((l >> 4) & 3) * 32768 + (l & 15) * 16, WS_H1 + 1048576 + kvh * 131072 + w * 8192 + j * 256);
  }
  SB;
  ST(10);
  {
    const unsigned soml = WS_ML + b * 16384 + kvh * 2048 + (s >> 2) * 512 + (w >> 2) * 256;
    const unsigned voml = (((l & 15) + (l >> 4) + 4 * (w & 3)) & 15) * 16;
    const unsigned soh = WS_H1 + b * 1048576 + kvh * 131072 + s * 8192 + w * 1024;
    u32x4 gm = ld_sc1(rws, voml, soml);
    u32x4 gd = ld_sc1(rws, l * 16, soh);
    SB;
    if (!__all(gm[1] == tag && gm[3] == tag)) {
      for (int it = 0; it < SPIN_CAP; ++it) {
      CLOB;
        gm = ld_sc1(rws, voml, soml);
        if (__all(gm[1] == tag && gm[3] == tag)) break;
      }
    }
    const float mm = asf(gm[0]);
    float M = fmaxf(mm, dpp<ROR(1)>(mm));
    M = fmaxf(M, dpp<ROR(2)>(M));
    M = fmaxf(M, dpp<ROR(4)>(M));
    M = fmaxf(M, dpp<ROR(8)>(M));
    const float e = ex2(mm - M);
    float L = e * asf(gm[2]);
    L += dpp<ROR(1)>(L);
    L += dpp<ROR(2)>(L);
    L += dpp<ROR(4)>(L);
    L += dpp<ROR(8)>(L);
    const float wgt = dpp<NBC(0)>(e / L);
    SB;
    if (!__all(gd[1] == tag && gd[3] == tag)) {
      for (int it = 0; it < SPIN_CAP; ++it) {
      CLOB;
        gd = ld_sc1(rws, l * 16, soh);
        if (__all(gd[1] == tag && gd[3] == tag)) break;
      }
    }
    float v = add32(asf(gd[0]) * wgt, asf(gd[2]) * wgt);
    v = add16(v, v);
    const int row = 16 * (l & 1) + 8 * (l >> 5) + 4 * ((l >> 1) & 1) + ((l >> 2) & 3);
    sm.o2[w >> 2][row * 4 + (w & 3)] = v;
  }
  ST(11);
  bar();
  {
    f32x4 lo = {0.f, 0.f, 0.f, 0.f}, hi = lo;
#pragma unroll
    for (int i = 0; i < 4; ++i) {
      const f32x4 t = *(const f32x4*)&sm.o2[(l >> 2) & 1][(((i + s) & 3) * 8 + (l >> 4) * 2 + ((l >> 3) & 1)) * 4];
      const float o = (t[0] + t[1]) + (t[2] + t[3]);
      const unsigned pb = pk(o, o);
      const unsigned x0_ = pb & m0, x1_ = pb & m1;
      lo = mfma16(wo[i][0], wo[i][1], x0_, x1_, lo);
      hi = mfma16(wo[i][2], wo[i][3], x0_, x1_, hi);
    }
    float r[4];
#pragma unroll
    for (int i = 0; i < 4; ++i) {
      const u32x2 t = sw32(lo[i], hi[i]);
      r[i] = i < 2 ? asf(t[0]) + dpp<ROR(8)>(asf(t[1])) : asf(t[1]) + dpp<ROR(8)>(asf(t[0]));
    }
    const bool up = l & 8;
    st_sc1(u32x4{asu(up ? r[2] : r[0]), tag, asu(up ? r[3] : r[1]), tag}, rws, (l >> 2) * 8192 + (l & 3) * 16,
           WS_H2 + b * 1048576 + w * 131072 + (kvh * 16 + s) * 64);
  }
  SB;
  ST(12);
  if (w == 0) {
    const int ci = kvh * 16 + s;
    const unsigned so = WS_H2 + b * 1048576 + ci * 8192;
    u32x4 gg[8];
    for (int it = 0; it < SPIN_CAP; ++it) {
      CLOB;
#pragma unroll
      for (int i = 0; i < 8; ++i) gg[i] = ld_sc1(rws, l * 16 + (i & 3) * 1024, so + (i >> 2) * 4096);
      bool ok = true;
#pragma unroll
      for (int i = 0; i < 8; ++i) ok = ok && gg[i][1] == tag && gg[i][3] == tag;
      if (__all(ok)) break;
    }
    const float v0 = ((asf(gg[0][0]) + asf(gg[1][0])) + (asf(gg[2][0]) + asf(gg[3][0]))) + ((asf(gg[4][0]) + asf(gg[5][0])) + (asf(gg[6][0]) + asf(gg[7][0])));
    const float v1 = ((asf(gg[0][2]) + asf(gg[1][2])) + (asf(gg[2][2]) + asf(gg[3][2]))) + ((asf(gg[4][2]) + asf(gg[5][2])) + (asf(gg[6][2]) + asf(gg[7][2])));
    float v = add32(v0, v1);
    v = add16(v, v);
    v += dpp<ROR(8)>(v);
    v += dpp<ROR(4)>(v);
    if ((l & 0x1c) == 0) {
      const int col = 64 * (ci >> 4) + 32 * ((ci >> 2) & 1) + 16 * ((ci >> 1) & 1) + 8 * (l >> 5) + 4 * ((ci >> 3) & 1) + (l & 3);
      ((u16*)out)[(ci & 1) * 1024 + b * 512 + col] = (u16)pk(v, v);
      if (blk == 0 && l == 0) __builtin_amdgcn_raw_buffer_store_b32(tag, rws, 0, WS_EPOCH, AUX_SC1);
    }
  }
  ST(13);
  if (blk == DLY_B && w == 0) {
    unsigned long long dt = stm[DLY_J] - stm[DLY_I];
    if (dt > 10000ull) dt = 10000ull;
    const unsigned long long e = stm[13] + DLY_K * dt;
    for (int it = 0; it < 10000000; ++it) { if (rt() >= e) break; }
  }
}

extern "C" void kernel_launch(void* const* d_in, const int* in_sizes, int n_in, void* d_out, int out_size, void* d_ws,
                              size_t ws_size, hipStream_t stream) {
  fused_decode<<<256, 512, 0, stream>>>((const u8*)d_in[0], (const u8*)d_in[1], (const u8*)d_in[2], (const u8*)d_in[3],
                                        (const u8*)d_in[4], (const u8*)d_in[5], (const u8*)d_in[6], (const u8*)d_in[7],
                                        (const u8*)d_in[8], (const u8*)d_in[9], (u8*)d_out, (u8*)d_ws);
}
```

```cpp
#include <hip/hip_runtime.h>
#include <hip/hip_bf16.h>
#include <stdint.h>

typedef unsigned char u8;
typedef unsigned short u16;
typedef __attribute__((ext_vector_type(4))) unsigned u32x4;
typedef __attribute__((ext_vector_type(2))) unsigned u32x2;
typedef __attribute__((ext_vector_type(4))) float f32x4;
typedef __attribute__((ext_vector_type(2))) float f32x2;
typedef __attribute__((ext_vector_type(8))) __bf16 bf16x8;
typedef __attribute__((ext_vector_type(2))) __bf16 bf16x2;
typedef __attribute__((ext_vector_type(4))) short s16x4;
typedef __amdgpu_buffer_rsrc_t rsrc_t;

#define SB __builtin_amdgcn_sched_barrier(0)
#define CLOB asm volatile("" ::: "memory")
#define WVM(n) do { SB; __builtin_amdgcn_s_waitcnt(((n) & 15) | (((n) >> 4) << 14) | 0x0f70); SB; } while (0)
#define DEVI __device__ __forceinline__

#define WS_EPOCH 0u
#define WS_Q     4096u
#define WS_ML    (WS_Q + 65536u)
#define WS_H1    (WS_ML + 32768u)
#define WS_H2    (WS_H1 + 2097152u)
#define WS_END   (WS_H2 + 2097152u)
#define SPIN_CAP 100000

#define AUX_NT  2
#define AUX_SC1 16

DEVI rsrc_t mk(const void* p, unsigned bytes) {
  return __builtin_amdgcn_make_buffer_rsrc((void*)p, (short)0, (int)bytes, 0x00020000);
}
DEVI u32x4 ld_nt(rsrc_t r, unsigned voff, unsigned soff) {
  return __builtin_amdgcn_raw_buffer_load_b128(r, (int)voff, (int)soff, AUX_NT);
}
DEVI u32x4 ld_sc1(rsrc_t r, unsigned voff, unsigned soff) {
  return __builtin_amdgcn_raw_buffer_load_b128(r, (int)voff, (int)soff, AUX_SC1);
}
DEVI u32x2 ld8_sc1(rsrc_t r, unsigned voff, unsigned soff) {
  return __builtin_amdgcn_raw_buffer_load_b64(r, (int)voff, (int)soff, AUX_SC1);
}
DEVI void st_sc1(u32x4 v, rsrc_t r, unsigned voff, unsigned soff) {
  __builtin_amdgcn_raw_buffer_store_b128(v, r, (int)voff, (int)soff, AUX_SC1);
}
DEVI float asf(unsigned v) { return __uint_as_float(v); }
DEVI unsigned asu(float v) { return __float_as_uint(v); }
DEVI float blo(unsigned v) { return asf(v << 16); }
DEVI float bhi(unsigned v) { return asf(v & 0xffff0000u); }
DEVI unsigned pk(float a, float b) {
  f32x2 f = {a, b};
  bf16x2 h = __builtin_convertvector(f, bf16x2);
  return __builtin_bit_cast(unsigned, h);
}
template <int CTRL> DEVI float dpp(float v) {
  return asf((unsigned)__builtin_amdgcn_update_dpp(0, (int)asu(v), CTRL, 0xf, 0xf, true));
}
template <int CTRL> DEVI unsigned dppu(unsigned v) {
  return (unsigned)__builtin_amdgcn_update_dpp(0, (int)v, CTRL, 0xf, 0xf, true);
}
#define QP(a, b, c, d) ((a) | ((b) << 2) | ((c) << 4) | ((d) << 6))
#define ROR(n) (0x120 + (n))
#define NBC(n) (0x150 + (n))
DEVI u32x2 sw32(float a, float b) { auto r = __builtin_amdgcn_permlane32_swap(asu(a), asu(b), false, false); return u32x2{r[0], r[1]}; }
DEVI u32x2 sw16(float a, float b) { auto r = __builtin_amdgcn_permlane16_swap(asu(a), asu(b), false, false); return u32x2{r[0], r[1]}; }
DEVI float add32(float a, float b) { u32x2 r = sw32(a, b); return asf(r[0]) + asf(r[1]); }
DEVI float add16(float a, float b) { u32x2 r = sw16(a, b); return asf(r[0]) + asf(r[1]); }
DEVI float max32(float a) { u32x2 r = sw32(a, a); return fmaxf(asf(r[0]), asf(r[1])); }
DEVI float max16(float a) { u32x2 r = sw16(a, a); return fmaxf(asf(r[0]), asf(r[1])); }
DEVI float ex2(float x) { return __builtin_amdgcn_exp2f(x); }
DEVI f32x4 mfma32(u32x4 a, u32x4 b, f32x4 c) {
  return __builtin_amdgcn_mfma_f32_16x16x32_bf16(__builtin_bit_cast(bf16x8, a), __builtin_bit_cast(bf16x8, b), c, 0, 0, 0);
}
DEVI f32x4 mfma16(unsigned a0, unsigned a1, unsigned b0, unsigned b1, f32x4 c) {
  u32x2 a = {a0, a1}, b = {b0, b1};
  return __builtin_amdgcn_mfma_f32_16x16x16bf16_1k(__builtin_bit_cast(s16x4, a), __builtin_bit_cast(s16x4, b), c, 0, 0, 0);
}
DEVI void bar() { __builtin_amdgcn_fence(__ATOMIC_RELEASE, "workgroup", "local"); __builtin_amdgcn_s_barrier(); __builtin_amdgcn_fence(__ATOMIC_ACQUIRE, "workgroup", "local"); }

#define ST(i)

DEVI float qk_group(const u32x4* k, const u32x4* qf, bool odd) {
  f32x4 d = {0.f, 0.f, 0.f, 0.f};
  d = mfma32(k[0], qf[0], d);
  d = mfma32(k[1], qf[1], d);
  d = mfma32(k[2], qf[2], d);
  d = mfma32(k[3], qf[3], d);
  float a = odd ? d[3] : d[0], b = odd ? d[1] : d[2];
  float u = a + dpp<QP(1, 0, 3, 2)>(b);
  u = blo(pk(u, u)) * 0.08838834764831845f;
  return blo(pk(u, u));
}
DEVI float head_max(float m) {
  m = fmaxf(m, dpp<QP(1, 0, 3, 2)>(m));
  m = fmaxf(m, dpp<QP(2, 3, 0, 1)>(m));
  return max32(max16(m));
}
DEVI float head_sum(float m) {
  m += dpp<QP(1, 0, 3, 2)>(m);
  m += dpp<QP(2, 3, 0, 1)>(m);
  m = add16(m, m);
  return add32(m, m);
}
template <int I> DEVI void pv_one(u32x4 v, unsigned P, unsigned m0, unsigned m1, f32x4& lo, f32x4& hi) {
  unsigned pb = dppu<QP(I, I, I, I)>(P);
  unsigned x0 = pb & m0, x1 = pb & m1;
  lo = mfma16(v[0], v[1], x0, x1, lo);
  hi = mfma16(v[2], v[3], x0, x1, hi);
}
DEVI void pv_group(const u32x4* v, unsigned P, unsigned m0, unsigned m1, f32x4& lo, f32x4& hi) {
  pv_one<0>(v[0], P, m0, m1, lo, hi);
  pv_one<1>(v[1], P, m0, m1, lo, hi);
  pv_one<2>(v[2], P, m0, m1, lo, hi);
  pv_one<3>(v[3], P, m0, m1, lo, hi);
}

struct Smem {
  f32x4 qred[8][64];
  unsigned qs[2][4][64];
  f32x4 acc[2][8][128];
  f32x2 ml[2][4][8];
  float o2[2][160];
};

__global__ void __launch_bounds__(512, 2)
fused_decode(const u8* __restrict__ xA, const u8* __restrict__ kA, const u8* __restrict__ vA, const u8* __restrict__ wqA,
             const u8* __restrict__ woA, const u8* __restrict__ xB, const u8* __restrict__ kB, const u8* __restrict__ vB,
             const u8* __restrict__ wqB, const u8* __restrict__ woB, u8* __restrict__ out, u8* ws
) {
  __shared__ Smem sm;
  ST(0);
  const int tid = threadIdx.x, l = tid & 63, w = __builtin_amdgcn_readfirstlane(tid >> 6);
  const int blk = blockIdx.x, kvh = blk & 7, j = blk >> 3, b = j >> 4, s = j & 15, g = b * 8 + kvh;
  const float LOG2E = 1.4426950408889634f;

  const rsrc_t rws = mk(ws, WS_END);
  const rsrc_t rx = mk(b ? xB : xA, 2048);
  const rsrc_t rwq = mk(b ? wqB : wqA, 4194304);
  const rsrc_t rwo = mk((b ? woB : woA) + (size_t)(kvh * 512 + s * 32) * 1024, 32768);
  const rsrc_t rkA = mk(kA + ((size_t)g * 8192 + 6144) * 256, 524288);
  const rsrc_t rvA = mk(vA + ((size_t)g * 8192 + 6144) * 256, 524288);
  const rsrc_t rkB = mk(kB + (size_t)g * 8192 * 256, 2097152);
  const rsrc_t rvB = mk(vB + (size_t)g * 8192 * 256, 2097152);

  const unsigned ep = __builtin_amdgcn_raw_buffer_load_b32(rws, 0, WS_EPOCH, AUX_SC1);
  const u32x2 x0 = __builtin_amdgcn_raw_buffer_load_b64(rx, (l >> 2) * 8, w * 128, 0);
  const u32x2 x1 = __builtin_amdgcn_raw_buffer_load_b64(rx, (l >> 2) * 8 + 1024, w * 128, 0);
  SB;
  u32x4 wq[4], ka[4], va[4], kb[16], vb[16], wo[4];
  {
    const unsigned vo = (l >> 2) * 32768 + (l & 3) * 16, so = w * 524288 + kvh * 1024 + s * 64;
#pragma unroll
    for (int i = 0; i < 4; ++i) { wq[i] = ld_nt(rwq, vo, so + i * 8192); SB; }
  }
  const unsigned vok = (4 * ((l >> 1) & 1) + ((l >> 2) & 3)) * 256 + (l >> 4) * 32 + (l & 1) * 16, vov = l * 16;
#define SOA(t) ((unsigned)((s * 16 + w * 2 + (t) + 17 * g) & 255) * 2048u)
#define SOB(t) ((unsigned)((s * 64 + w * 8 + (t) + 65 * g) & 1023) * 2048u)
#pragma unroll
  for (int i = 0; i < 4; ++i) { ka[i] = ld_nt(rkA, vok + (i & 1) * 128, SOA(i >> 1)); SB; }
#define LDK(a, e) _Pragma("unroll") for (int i = a; i < e; ++i) { kb[i] = ld_nt(rkB, vok + (i & 1) * 128, SOB(i >> 1)); SB; }
#define LDV(a, e) _Pragma("unroll") for (int i = a; i < e; ++i) { vb[i] = ld_nt(rvB, vov + (i & 1) * 1024, SOB(i >> 1)); SB; }

  {
    float a[16];
#pragma unroll
    for (int i = 0; i < 16; ++i) a[i] = 0.f;
#pragma unroll
    for (int i = 0; i < 4; ++i) {
      const float xa = (i & 1) ? bhi(x0[i >> 1]) : blo(x0[i >> 1]), xb = (i & 1) ? bhi(x1[i >> 1]) : blo(x1[i >> 1]);
#pragma unroll
      for (int c = 0; c < 4; ++c) {
        const float e0 = blo(wq[i][c]), e1 = bhi(wq[i][c]);
        a[2 * c] = fmaf(e0, xa, a[2 * c]);
        a[2 * c + 1] = fmaf(e1, xa, a[2 * c + 1]);
        a[8 + 2 * c] = fmaf(e0, xb, a[8 + 2 * c]);
        a[8 + 2 * c + 1] = fmaf(e1, xb, a[8 + 2 * c + 1]);
      }
    }
#pragma unroll
    for (int i = 0; i < 8; ++i) a[i] = add32(a[i], a[i + 8]);
#pragma unroll
    for (int i = 0; i < 4; ++i) a[i] = add16(a[i], a[i + 4]);
    sm.qred[w][l] = f32x4{a[0], a[1], a[2], a[3]};
  }
  const unsigned tag = __builtin_amdgcn_readfirstlane(ep) + 1u;
  ST(1);
  bar();
  if (w == 0) {
    f32x4 t = sm.qred[0][l];
#pragma unroll
    for (int i = 1; i < 8; ++i) t += sm.qred[i][l];
#pragma unroll
    for (int i = 0; i < 4; ++i) { t[i] += dpp<ROR(4)>(t[i]); t[i] += dpp<ROR(8)>(t[i]); }
    if ((l & 12) == 0) {
      u32x4 gq = {pk(t[0], t[1]), tag, pk(t[2], t[3]), tag};
      st_sc1(gq, rws, (l >> 5) * 16384 + (l & 3) * 32 + ((l >> 4) & 1) * 16, WS_Q + b * 32768 + kvh * 2048 + s * 128);
    }
  }
  SB;
  ST(2);
#pragma unroll
  for (int i = 0; i < 4; ++i) { va[i] = ld_nt(rvA, vov + (i & 1) * 1024, SOA(i >> 1)); SB; }
  LDK(0, 4)
  WVM(8);
  LDK(4, 8)
  const unsigned soq = WS_Q + (w >> 2) * 32768 + b * 16384 + kvh * 2048 + (w & 3) * 512;
  u32x2 qg = ld8_sc1(rws, l * 8, soq);
  WVM(9);
  LDK(8, 12)
  WVM(9);
  LDK(12, 16)
  if (!__all(qg[1] == tag)) {
    for (int it = 0; it < SPIN_CAP; ++it) {
      CLOB;
      qg = ld8_sc1(rws, l * 8, soq);
      if (__all(qg[1] == tag)) break;
    }
  }
  sm.qs[w >> 2][w & 3][l] = qg[0];
  SB;
  LDV(0, 4)
  ST(3);
  bar();
  ST(4);

  const bool odd = l & 1;
  const int h = (l >> 2) & 3;
  const unsigned m0 = (l & 2) ? 0u : (odd ? 0xffff0000u : 0xffffu), m1 = (l & 2) ? (odd ? 0xffff0000u : 0xffffu) : 0u;
  const unsigned t0 = (l & 2) ? 0u : ~0u;
  u32x4 qf[4];
  {
    const u32x4 q0 = *(const u32x4*)&sm.qs[0][h][(l >> 4) * 8 + (l & 1) * 4], q1 = *(const u32x4*)&sm.qs[0][h][32 + (l >> 4) * 8 + (l & 1) * 4];
    qf[0] = q0 & t0; qf[1] = q1 & t0; qf[2] = q0 & ~t0; qf[3] = q1 & ~t0;
    const float u = qk_group(ka, qf, odd);
    const float m2 = head_max(u) * LOG2E;
    const float p = ex2(fmaf(u, LOG2E, -m2));
    const float ls = head_sum(p);
    f32x4 lo = {0.f, 0.f, 0.f, 0.f}, hi = lo;
    pv_group(va, pk(p, p), m0, m1, lo, hi);
    sm.acc[0][w][2 * l] = lo; sm.acc[0][w][2 * l + 1] = hi;
    if ((l & 0x33) == 0) sm.ml[0][h][w] = f32x2{m2, ls};
  }
  SB;
  ST(5);
  unsigned P[4];
  float m2B;
  {
    const u32x4 q0 = *(const u32x4*)&sm.qs[1][h][(l >> 4) * 8 + (l & 1) * 4], q1 = *(const u32x4*)&sm.qs[1][h][32 + (l >> 4) * 8 + (l & 1) * 4];
    qf[0] = q0 & t0; qf[1] = q1 & t0; qf[2] = q0 & ~t0; qf[3] = q1 & ~t0;
    float u[4];
    u[0] = qk_group(kb, qf, odd);
    u[1] = qk_group(kb + 4, qf, odd);
    SB;
    LDV(4, 8)
    u[2] = qk_group(kb + 8, qf, odd);
    SB;
    LDV(8, 12)
    u[3] = qk_group(kb + 12, qf, odd);
    SB;
    LDV(12, 16)
    m2B = head_max(fmaxf(fmaxf(u[0], u[1]), fmaxf(u[2], u[3]))) * LOG2E;
    float ls = 0.f;
#pragma unroll
    for (int i = 0; i < 4; ++i) { const float p = ex2(fmaf(u[i], LOG2E, -m2B)); ls += p; P[i] = pk(p, p); }
    ls = head_sum(ls);
    if ((l & 0x33) == 0) sm.ml[1][h][w] = f32x2{m2B, ls};
  }
  ST(6);
  bar();
  ST(7);
  float fB;
  {
    const f32x4* mp = (const f32x4*)&sm.ml[1][h][0];
    const f32x4 a0 = mp[0], a1 = mp[1], a2 = mp[2], a3 = mp[3];
    const float M = fmaxf(fmaxf(fmaxf(a0[0], a0[2]), fmaxf(a1[0], a1[2])), fmaxf(fmaxf(a2[0], a2[2]), fmaxf(a3[0], a3[2])));
    fB = ex2(m2B - M);
  }
  if (w < 4) {
    const int hh = (l >> 4) & 3;
    float mw[8], M = -INFINITY;
#pragma unroll
    for (int i = 0; i < 8; ++i) { mw[i] = sm.ml[0][hh][i][0]; M = fmaxf(M, mw[i]); }
    f32x2 t = {0.f, 0.f};
#pragma unroll
    for (int i = 0; i < 8; ++i) t += ((const f32x2*)&sm.acc[0][i][0])[tid] * ex2(mw[i] - M);
    st_sc1(u32x4{asu(t[0]), tag, asu(t[1]), tag}, rws, hh * 32768 + (l & 15) * 16, WS_H1 + kvh * 131072 + w * 8192 + j * 256);
  } else if (w == 4) {
    const f32x2 v = sm.ml[(l >> 5) & 1][(l >> 3) & 3][l & 7];
    float M = v[0];
    M = fmaxf(M, dpp<QP(1, 0, 3, 2)>(M));
    M = fmaxf(M, dpp<QP(2, 3, 0, 1)>(M));
    M = fmaxf(M, dpp<0x141>(M));
    float L = v[1] * ex2(v[0] - M);
    L += dpp<QP(1, 0, 3, 2)>(L);
    L += dpp<QP(2, 3, 0, 1)>(L);
    L += dpp<0x141>(L);
    if ((l & 7) == 0) st_sc1(u32x4{asu(M), tag, asu(L), tag}, rws, (l >> 5) * 16384 + ((l >> 3) & 3) * 512, WS_ML + kvh * 2048 + j * 16);
  }
  SB;
  ST(8);
  {
    f32x4 lo = {0.f, 0.f, 0.f, 0.f}, hi = lo;
    pv_group(vb, P[0], m0, m1, lo, hi);
    WVM(12);
    {
      const unsigned vo = (l >> 3) * 1024 + (l & 7) * 16;
#pragma unroll
      for (int i = 0; i < 4; ++i) { wo[i] = ld_nt(rwo, vo, ((i + s) & 3) * 8192 + w * 128); SB; }
    }
#pragma unroll
    for (int G = 1; G < 4; ++G) pv_group(vb + 4 * G, P[G], m0, m1, lo, hi);
    sm.acc[1][w][2 * l] = lo * fB; sm.acc[1][w][2 * l + 1] = hi * fB;
  }
  ST(9);
  bar();
  if (w < 4) {
    f32x2 t[8];
#pragma unroll
    for (int i = 0; i < 8; ++i) t[i] = ((const f32x2*)&sm.acc[1][i][0])[tid];
    const f32x2 r = ((t[0] + t[1]) + (t[2] + t[3])) + ((t[4] + t[5]) + (t[6] + t[7]));
    st_sc1(u32x4{asu(r[0]), tag, asu(r[1]), tag}, rws, ((l >> 4) & 3) * 32768 + (l & 15) * 16, WS_H1 + 1048576 + kvh * 131072 + w * 8192 + j * 256);
  }
  SB;
  ST(10);
  {
    const unsigned soml = WS_ML + b * 16384 + kvh * 2048 + (s >> 2) * 512 + (w >> 2) * 256;
    const unsigned voml = (((l & 15) + (l >> 4) + 4 * (w & 3)) & 15) * 16;
    const unsigned soh = WS_H1 + b * 1048576 + kvh * 131072 + s * 8192 + w * 1024;
    u32x4 gm = ld_sc1(rws, voml, soml);
    u32x4 gd = ld_sc1(rws, l * 16, soh);
    SB;
    if (!__all(gm[1] == tag && gm[3] == tag)) {
      for (int it = 0; it < SPIN_CAP; ++it) {
      CLOB;
        gm = ld_sc1(rws, voml, soml);
        if (__all(gm[1] == tag && gm[3] == tag)) break;
      }
    }
    const float mm = asf(gm[0]);
    float M = fmaxf(mm, dpp<ROR(1)>(mm));
    M = fmaxf(M, dpp<ROR(2)>(M));
    M = fmaxf(M, dpp<ROR(4)>(M));
    M = fmaxf(M, dpp<ROR(8)>(M));
    const float e = ex2(mm - M);
    float L = e * asf(gm[2]);
    L += dpp<ROR(1)>(L);
    L += dpp<ROR(2)>(L);
    L += dpp<ROR(4)>(L);
    L += dpp<ROR(8)>(L);
    const float wgt = dpp<NBC(0)>(e / L);
    SB;
    if (!__all(gd[1] == tag && gd[3] == tag)) {
      for (int it = 0; it < SPIN_CAP; ++it) {
      CLOB;
        gd = ld_sc1(rws, l * 16, soh);
        if (__all(gd[1] == tag && gd[3] == tag)) break;
      }
    }
    float v = add32(asf(gd[0]) * wgt, asf(gd[2]) * wgt);
    v = add16(v, v);
    const int row = 16 * (l & 1) + 8 * (l >> 5) + 4 * ((l >> 1) & 1) + ((l >> 2) & 3);
    sm.o2[w >> 2][row * 4 + (w & 3)] = v;
  }
  ST(11);
  bar();
  {
    f32x4 lo = {0.f, 0.f, 0.f, 0.f}, hi = lo;
#pragma unroll
    for (int i = 0; i < 4; ++i) {
      const f32x4 t = *(const f32x4*)&sm.o2[(l >> 2) & 1][(((i + s) & 3) * 8 + (l >> 4) * 2 + ((l >> 3) & 1)) * 4];
      const float o = (t[0] + t[1]) + (t[2] + t[3]);
      const unsigned pb = pk(o, o);
      const unsigned x0_ = pb & m0, x1_ = pb & m1;
      lo = mfma16(wo[i][0], wo[i][1], x0_, x1_, lo);
      hi = mfma16(wo[i][2], wo[i][3], x0_, x1_, hi);
    }
    float r[4];
#pragma unroll
    for (int i = 0; i < 4; ++i) {
      const u32x2 t = sw32(lo[i], hi[i]);
      r[i] = i < 2 ? asf(t[0]) + dpp<ROR(8)>(asf(t[1])) : asf(t[1]) + dpp<ROR(8)>(asf(t[0]));
    }
    const bool up = l & 8;
    st_sc1(u32x4{asu(up ? r[2] : r[0]), tag, asu(up ? r[3] : r[1]), tag}, rws, (l >> 2) * 8192 + (l & 3) * 16,
           WS_H2 + b * 1048576 + w * 131072 + (kvh * 16 + s) * 64);
  }
  SB;
  ST(12);
  if (w == 0) {
    const int ci = kvh * 16 + s;
    const unsigned so = WS_H2 + b * 1048576 + ci * 8192;
    u32x4 gg[8];
    for (int it = 0; it < SPIN_CAP; ++it) {
      CLOB;
#pragma unroll
      for (int i = 0; i < 8; ++i) gg[i] = ld_sc1(rws, l * 16 + (i & 3) * 1024, so + (i >> 2) * 4096);
      bool ok = true;
#pragma unroll
      for (int i = 0; i < 8; ++i) ok = ok && gg[i][1] == tag && gg[i][3] == tag;
      if (__all(ok)) break;
    }
    const float v0 = ((asf(gg[0][0]) + asf(gg[1][0])) + (asf(gg[2][0]) + asf(gg[3][0]))) + ((asf(gg[4][0]) + asf(gg[5][0])) + (asf(gg[6][0]) + asf(gg[7][0])));
    const float v1 = ((asf(gg[0][2]) + asf(gg[1][2])) + (asf(gg[2][2]) + asf(gg[3][2]))) + ((asf(gg[4][2]) + asf(gg[5][2])) + (asf(gg[6][2]) + asf(gg[7][2])));
    float v = add32(v0, v1);
    v = add16(v, v);
    v += dpp<ROR(8)>(v);
    v += dpp<ROR(4)>(v);
    if ((l & 0x1c) == 0) {
      const int col = 64 * (ci >> 4) + 32 * ((ci >> 2) & 1) + 16 * ((ci >> 1) & 1) + 8 * (l >> 5) + 4 * ((ci >> 3) & 1) + (l & 3);
      __builtin_amdgcn_raw_buffer_store_b16((u16)pk(v, v), mk(out, 4096), ((ci & 1) * 1024 + b * 512 + col) * 2, 0, AUX_SC1);
      if (blk == 0 && l == 0) __builtin_amdgcn_raw_buffer_store_b32(tag, rws, 0, WS_EPOCH, AUX_SC1);
    }
  }
}

extern "C" void kernel_launch(void* const* d_in, const int* in_sizes, int n_in, void* d_out, int out_size, void* d_ws,
                              size_t ws_size, hipStream_t stream) {
  fused_decode<<<256, 512, 0, stream>>>((const u8*)d_in[0], (const u8*)d_in[1], (const u8*)d_in[2], (const u8*)d_in[3],
                                        (const u8*)d_in[4], (const u8*)d_in[5], (const u8*)d_in[6], (const u8*)d_in[7],
                                        (const u8*)d_in[8], (const u8*)d_in[9], (u8*)d_out, (u8*)d_ws);
}
```

```cpp
#define G 1
#define NT 512
#define LDSB 0
#define ALLW 1
#define DLY 700000ull
#include <hip/hip_runtime.h>
#include <hip/hip_bf16.h>
typedef unsigned short u16;
typedef __attribute__((ext_vector_type(4))) unsigned u32x4;
__device__ __forceinline__ unsigned long long rt() { unsigned long long t; asm volatile("s_memrealtime %0\n\ts_waitcnt lgkmcnt(0)" : "=s"(t)::"memory"); return t; }
__device__ __forceinline__ float b2f(u16 v) { return __uint_as_float((unsigned)v << 16); }
__device__ __forceinline__ float rb(float f) { unsigned u = __float_as_uint(f); u += 0x7fff + ((u >> 16) & 1); return __uint_as_float(u & 0xffff0000u); }
__global__ void __launch_bounds__(NT) slowk(const u16* xA, const u16* kA, const u16* vA, const u16* wqA, const u16* woA, const u16* xB, const u16* kB, const u16* vB, const u16* wqB, const u16* woB, u16* out, float* ws) {
  const unsigned long long t0 = rt();
#ifdef NV
  asm volatile("v_mov_b32 v" NV ", 0" ::: "v" NV);
#endif
  if (blockIdx.x == 0) {
    float* q = ws; float* o = ws + 4096; float* p = ws + 8192; float* red = ws + 65536;
    const int t = threadIdx.x;
    for (int br = 0; br < 2; ++br) for (int b = 0; b < 2; ++b) {
      const u16 *x = br ? xB : xA, *K = br ? kB : kA, *V = br ? vB : vA, *wq = br ? wqB : wqA, *wo = br ? woB : woA;
      const int k0 = br ? 0 : 6144;
      for (int c = t; c < 4096; c += NT) { float a = 0; for (int r = 0; r < 512; ++r) a += b2f(x[b * 512 + r]) * b2f(wq[(size_t)r * 4096 + c]); q[c] = rb(a); }
      __syncthreads();
      for (int kv = 0; kv < 8; ++kv) {
        const size_t g = (size_t)(b * 8 + kv) * 8192;
        __shared__ float qs[512];
        if (t < 512) qs[t] = q[kv * 512 + t];
        __syncthreads();
        float m[4] = {-1e30f, -1e30f, -1e30f, -1e30f};
        for (int k = k0 + t; k < 8192; k += NT) {
          float a[4] = {0, 0, 0, 0};
          for (int d = 0; d < 128; d += 8) { u32x4 kk = *(const u32x4*)(K + (g + k) * 128 + d);
            for (int e = 0; e < 8; ++e) { float f = b2f((u16)(kk[e >> 1] >> (16 * (e & 1)))); for (int h = 0; h < 4; ++h) a[h] += f * qs[h * 128 + d + e]; } }
          for (int h = 0; h < 4; ++h) { float s = rb(rb(a[h]) * 0.08838834764831845f); p[h * 8192 + k] = s; m[h] = fmaxf(m[h], s); }
        }
        for (int h = 0; h < 4; ++h) { for (int o2 = 1; o2 < 64; o2 <<= 1) m[h] = fmaxf(m[h], __shfl_xor(m[h], o2)); red[h * 1024 + (t >> 6)] = m[h]; }
        __syncthreads();
        for (int h = 0; h < 4; ++h) { float M = -1e30f; for (int i = 0; i < NT / 64; ++i) M = fmaxf(M, red[h * 1024 + i]); m[h] = M; }
        __syncthreads();
        float l[4] = {0, 0, 0, 0};
        for (int k = k0 + t; k < 8192; k += NT) for (int h = 0; h < 4; ++h) { float e = __expf(p[h * 8192 + k] - m[h]); p[h * 8192 + k] = e; l[h] += e; }
        for (int h = 0; h < 4; ++h) { for (int o2 = 1; o2 < 64; o2 <<= 1) l[h] += __shfl_xor(l[h], o2); red[h * 1024 + (t >> 6)] = l[h]; }
        __syncthreads();
        for (int h = 0; h < 4; ++h) { float L = 0; for (int i = 0; i < NT / 64; ++i) L += red[h * 1024 + i]; l[h] = L; }
        __syncthreads();
        for (int k = k0 + t; k < 8192; k += NT) for (int h = 0; h < 4; ++h) p[h * 8192 + k] = rb(p[h * 8192 + k] / l[h]);
        __syncthreads();
        { const int dg = t & 15, kp = t >> 4, np = NT / 16; float a[32];
          for (int i = 0; i < 32; ++i) a[i] = 0.f;
#pragma unroll 4
          for (int k = k0 + kp; k < 8192; k += np) { u32x4 vv = *(const u32x4*)(V + (g + k) * 128 + dg * 8); float pp[4]; for (int h = 0; h < 4; ++h) pp[h] = p[h * 8192 + k];
            for (int e = 0; e < 8; ++e) { float f = b2f((u16)(vv[e >> 1] >> (16 * (e & 1)))); for (int h = 0; h < 4; ++h) a[h * 8 + e] += f * pp[h]; } }
          for (int i = 0; i < 32; ++i) { a[i] += __shfl_xor(a[i], 16); a[i] += __shfl_xor(a[i], 32); }
          if ((t & 63) < 16) for (int i = 0; i < 32; ++i) red[(t >> 6) * 512 + (i >> 3) * 128 + dg * 8 + (i & 7)] = a[i];
          __syncthreads();
          if (t < 512) { float s = 0; for (int i = 0; i < NT / 64; ++i) s += red[i * 512 + t]; o[kv * 512 + t] = rb(s); }
          __syncthreads(); }
      }
      for (int c = t; c < 512; c += NT) { float a = 0; for (int r = 0; r < 4096; ++r) a += o[r] * b2f(wo[(size_t)r * 512 + c]); out[b * 1024 + br * 512 + c] = (u16)(__float_as_uint(rb(a)) >> 16); }
      __syncthreads();
    }
  }
  if (threadIdx.x < 64 && (ALLW || blockIdx.x == 0)) { const unsigned long long e = t0 + DLY; for (int it = 0; it < 100000000; ++it) { if (rt() >= e) break; __builtin_amdgcn_s_sleep(1); } }
}
extern "C" void kernel_launch(void* const* d_in, const int* in_sizes, int n_in, void* d_out, int out_size, void* d_ws, size_t ws_size, hipStream_t stream) {
  slowk<<<G, NT, 0, stream>>>((const u16*)d_in[0], (const u16*)d_in[1], (const u16*)d_in[2], (const u16*)d_in[3], (const u16*)d_in[4], (const u16*)d_in[5], (const u16*)d_in[6], (const u16*)d_in[7], (const u16*)d_in[8], (const u16*)d_in[9], (u16*)d_out, (float*)d_ws);
}
```

```cpp
#include <hip/hip_runtime.h>
#include <hip/hip_bf16.h>
#include <stdint.h>

typedef unsigned char u8;
typedef unsigned short u16;
typedef __attribute__((ext_vector_type(4))) unsigned u32x4;
typedef __attribute__((ext_vector_type(2))) unsigned u32x2;
typedef __attribute__((ext_vector_type(4))) float f32x4;
typedef __attribute__((ext_vector_type(2))) float f32x2;
typedef __attribute__((ext_vector_type(8))) __bf16 bf16x8;
typedef __attribute__((ext_vector_type(2))) __bf16 bf16x2;
typedef __attribute__((ext_vector_type(4))) short s16x4;
typedef __amdgpu_buffer_rsrc_t rsrc_t;
#define LDS __attribute__((address_space(3)))

#define SB __builtin_amdgcn_sched_barrier(0)
#define CLOB asm volatile("" ::: "memory")
#define DEVI __device__ __forceinline__

#define WS_EPOCH 0u
#define WS_Q     4096u
#define WS_ML    (WS_Q + 524288u)
#define WS_H1    (WS_ML + 32768u)
#define WS_H2    (WS_H1 + 1048576u)
#define WS_END   (WS_H2 + 1048576u)
#define SPIN_CAP 100000

#define DROP 0x80000000u
#define AUX_NT  2
#define AUX_SC1 16

DEVI rsrc_t mk(const void* p, unsigned bytes) {
  return __builtin_amdgcn_make_buffer_rsrc((void*)p, (short)0, (int)bytes, 0x00020000);
}
DEVI u32x4 ld_nt(rsrc_t r, unsigned voff, unsigned soff) {
  return __builtin_amdgcn_raw_buffer_load_b128(r, (int)voff, (int)soff, AUX_NT);
}
DEVI u32x4 ld_sc1(rsrc_t r, unsigned voff, unsigned soff) {
  return __builtin_amdgcn_raw_buffer_load_b128(r, (int)voff, (int)soff, AUX_SC1);
}
DEVI void st_sc1(u32x4 v, rsrc_t r, unsigned voff, unsigned soff) {
  __builtin_amdgcn_raw_buffer_store_b128(v, r, (int)voff, (int)soff, AUX_SC1);
}
DEVI float asf(unsigned v) { return __uint_as_float(v); }
DEVI unsigned asu(float v) { return __float_as_uint(v); }
DEVI float blo(unsigned v) { return asf(v << 16); }
DEVI float bhi(unsigned v) { return asf(v & 0xffff0000u); }
DEVI unsigned pk(float a, float b) {
  f32x2 f = {a, b};
  bf16x2 h = __builtin_convertvector(f, bf16x2);
  return __builtin_bit_cast(unsigned, h);
}
template <int CTRL> DEVI float dpp(float v) {
  return asf((unsigned)__builtin_amdgcn_update_dpp(0, (int)asu(v), CTRL, 0xf, 0xf, true));
}
template <int CTRL> DEVI unsigned dppu(unsigned v) {
  return (unsigned)__builtin_amdgcn_update_dpp(0, (int)v, CTRL, 0xf, 0xf, true);
}
#define QP(a, b, c, d) ((a) | ((b) << 2) | ((c) << 4) | ((d) << 6))
#define ROR(n) (0x120 + (n))
#define NBC(n) (0x150 + (n))
DEVI u32x2 sw32(float a, float b) { auto r = __builtin_amdgcn_permlane32_swap(asu(a), asu(b), false, false); return u32x2{r[0], r[1]}; }
DEVI u32x2 sw16(float a, float b) { auto r = __builtin_amdgcn_permlane16_swap(asu(a), asu(b), false, false); return u32x2{r[0], r[1]}; }
DEVI float add32(float a, float b) { u32x2 r = sw32(a, b); return asf(r[0]) + asf(r[1]); }
DEVI float add16(float a, float b) { u32x2 r = sw16(a, b); return asf(r[0]) + asf(r[1]); }
DEVI float max32(float a) { u32x2 r = sw32(a, a); return fmaxf(asf(r[0]), asf(r[1])); }
DEVI float max16(float a) { u32x2 r = sw16(a, a); return fmaxf(asf(r[0]), asf(r[1])); }
DEVI float ex2(float x) { return __builtin_amdgcn_exp2f(x); }
DEVI f32x4 mfma32(u32x4 a, u32x4 b, f32x4 c) {
  return __builtin_amdgcn_mfma_f32_16x16x32_bf16(__builtin_bit_cast(bf16x8, a), __builtin_bit_cast(bf16x8, b), c, 0, 0, 0);
}
DEVI f32x4 mfma16(unsigned a0, unsigned a1, unsigned b0, unsigned b1, f32x4 c) {
  u32x2 a = {a0, a1}, b = {b0, b1};
  return __builtin_amdgcn_mfma_f32_16x16x16bf16_1k(__builtin_bit_cast(s16x4, a), __builtin_bit_cast(s16x4, b), c, 0, 0, 0);
}
DEVI void bar() { __builtin_amdgcn_fence(__ATOMIC_RELEASE, "workgroup", "local"); __builtin_amdgcn_s_barrier(); __builtin_amdgcn_fence(__ATOMIC_ACQUIRE, "workgroup", "local"); }

DEVI float qk_group(const u32x4* k, const u32x4* qf, bool odd) {
  f32x4 d = {0.f, 0.f, 0.f, 0.f};
  d = mfma32(k[0], qf[0], d);
  d = mfma32(k[1], qf[1], d);
  d = mfma32(k[2], qf[2], d);
  d = mfma32(k[3], qf[3], d);
  float a = odd ? d[3] : d[0], b = odd ? d[1] : d[2];
  float u = a + dpp<QP(1, 0, 3, 2)>(b);
  u = blo(pk(u, u)) * 0.08838834764831845f;
  return blo(pk(u, u));
}
DEVI float head_max(float m) {
  m = fmaxf(m, dpp<QP(1, 0, 3, 2)>(m));
  m = fmaxf(m, dpp<QP(2, 3, 0, 1)>(m));
  return max32(max16(m));
}
DEVI float head_sum(float m) {
  m += dpp<QP(1, 0, 3, 2)>(m);
  m += dpp<QP(2, 3, 0, 1)>(m);
  m = add16(m, m);
  return add32(m, m);
}
template <int I> DEVI void pv_one(u32x4 v, unsigned P, unsigned m0, unsigned m1, f32x4& lo, f32x4& hi) {
  unsigned pb = dppu<QP(I, I, I, I)>(P);
  unsigned x0 = pb & m0, x1 = pb & m1;
  lo = mfma16(v[0], v[1], x0, x1, lo);
  hi = mfma16(v[2], v[3], x0, x1, hi);
}
DEVI void pv_group(const u32x4* v, unsigned P, unsigned m0, unsigned m1, f32x4& lo, f32x4& hi) {
  pv_one<0>(v[0], P, m0, m1, lo, hi);
  pv_one<1>(v[1], P, m0, m1, lo, hi);
  pv_one<2>(v[2], P, m0, m1, lo, hi);
  pv_one<3>(v[3], P, m0, m1, lo, hi);
}

DEVI unsigned enc(float v, unsigned f, unsigned hi) { return __builtin_amdgcn_bitop3_b32(asu(v) + 0x200u, hi, f, 0xea); }
DEVI unsigned enc8(float v, unsigned f) { return ((asu(v) + 0x80u) & ~0xffu) | f; }
DEVI f32x4 poll4(u32x4 g, u32x4 f, rsrc_t r, unsigned voff, unsigned soff) {
  u32x4 x = g ^ f;
  for (int it = 0; ((x[0] | x[1] | x[2] | x[3]) & 0x3ffu) != 0 && it < SPIN_CAP; ++it) {
    CLOB;
    x = ld_sc1(r, voff, soff) ^ f;
  }
  return __builtin_bit_cast(f32x4, x);
}

#define KARGS(d, o, w) (const u8*)d[3], (const u8*)d[8], (const u8*)d[0], (const u8*)d[5], (u8*)w, (const u8*)d[1], (const u8*)d[2], \
                       (const u8*)d[6], (const u8*)d[7], (const u8*)d[4], (const u8*)d[9], (u8*)o
struct Smem {
  f32x4 qred[8][64];
  unsigned qs[2][4][64];
  f32x4 acc[2][8][128];
  f32x2 ml[2][4][8];
  float o2[2][80];
  f32x4 o3[8];
};

__global__ void __launch_bounds__(512, 2)
fused_decode(const u8* __restrict__ wqA, const u8* __restrict__ wqB, const u8* __restrict__ xA, const u8* __restrict__ xB, u8* ws,
             const u8* __restrict__ kA, const u8* __restrict__ vA, const u8* __restrict__ kB, const u8* __restrict__ vB,
             const u8* __restrict__ woA, const u8* __restrict__ woB, u8* __restrict__ out) {
  __shared__ Smem sm;
  const int tid = threadIdx.x, l = tid & 63, w = __builtin_amdgcn_readfirstlane(tid >> 6);
  const int blk = blockIdx.x, kvh = blk & 7, j = blk >> 3,
            b = j >> 4, s = j & 15, g = b * 8 + kvh;
  const float LOG2E = 1.4426950408889634f;
  unsigned vo = (l >> 4) * 32768 + (l & 15) * 16, so = (s & 3) * 1048576 + w * 131072 + kvh * 1024 + (s >> 2) * 256;
  unsigned vx = (l >> 4) * 8, sx = (s & 3) * 256 + w * 32, so1 = so + 8192, so2 = so + 16384, so3 = so + 24576;
  asm volatile("" : "+v"(vo), "+v"(vx), "+s"(so), "+s"(so1), "+s"(so2), "+s"(so3), "+s"(sx));
  SB;
  asm volatile("" :: "s"(wqA), "s"(wqB), "s"(xA), "s"(xB), "s"(ws), "s"(kA), "s"(vA), "s"(kB), "s"(vB), "s"(woA), "s"(woB), "s"(out));
  const rsrc_t rwq = mk(b ? wqB : wqA, 4194304);
  const rsrc_t rx = mk(b ? xB : xA, 2048);
  u32x4 wq[4], ka[4], va[4], kb[16], vb[16], wo[4];
  u32x2 x0, x1;
  x0 = __builtin_amdgcn_raw_buffer_load_b64(rx, vx, sx, 0);
  x1 = __builtin_amdgcn_raw_buffer_load_b64(rx, vx + 1024, sx, 0);
  SB;
  wq[0] = ld_nt(rwq, vo, so); SB;
  wq[1] = ld_nt(rwq, vo, so1); SB;
  wq[2] = ld_nt(rwq, vo, so2); SB;
  wq[3] = ld_nt(rwq, vo, so3); SB;
  unsigned ep;
  asm volatile("s_load_dword %0, %1, 0x0" : "=&s"(ep) : "s"(ws) : "memory");
  SB;
  const int bc = kvh >> 2, kc = ((kvh & 4) ^ (b * 4)) | ((kvh + 1 - b) & 3), ci = (kvh & 3) * 32 + j;
  const rsrc_t rws = mk(ws, WS_END);
  const rsrc_t rwo = mk((bc ? woB : woA) + (size_t)(kc * 512 + s * 32) * 1024, 32768);
  const rsrc_t rkA = mk(kA + ((size_t)g * 8192 + 6144) * 256, 524288);
  const rsrc_t rvA = mk(vA + ((size_t)g * 8192 + 6144) * 256, 524288);
  const rsrc_t rkB = mk(kB + (size_t)g * 8192 * 256, 2097152);
  const rsrc_t rvB = mk(vB + (size_t)g * 8192 * 256, 2097152);
  const unsigned vok = (4 * ((l >> 1) & 1) + ((l >> 2) & 3)) * 256 + (l >> 4) * 32 + (l & 1) * 16, vov = l * 16;
#define SOA(t) ((unsigned)((s * 16 + w * 2 + (t) + 17 * g) & 255) * 2048u)
#define SOB(t) ((unsigned)(((((w >> 2) * 2 + ((t) >> 2) + s) & 3) * 256) + ((s * 16 + (w & 3) * 4 + ((t) & 3) + 65 * g) & 255)) * 2048u)
  __builtin_amdgcn_s_barrier();
  SB;
#pragma unroll
  for (int i = 0; i < 4; ++i) { ka[i] = ld_nt(rkA, vok + (i & 1) * 128, SOA(i >> 1)); SB; }
#pragma unroll
  for (int i = 0; i < 4; ++i) { va[i] = ld_nt(rvA, vov + (i & 1) * 1024, SOA(i >> 1)); SB; }
#define LDK(a, e) _Pragma("unroll") for (int i = a; i < e; ++i) { kb[i] = ld_nt(rkB, vok + (i & 1) * 128, SOB(i >> 1)); SB; }
#define LDV(a, e) _Pragma("unroll") for (int i = a; i < e; ++i) { vb[i] = ld_nt(rvB, vov + (i & 1) * 1024, SOB(i >> 1)); SB; }

  {
    float a[16];
#pragma unroll
    for (int i = 0; i < 16; ++i) a[i] = 0.f;
#pragma unroll
    for (int i = 0; i < 4; ++i) {
      const float xa = (i & 1) ? bhi(x0[i >> 1]) : blo(x0[i >> 1]), xb = (i & 1) ? bhi(x1[i >> 1]) : blo(x1[i >> 1]);
#pragma unroll
      for (int c = 0; c < 4; ++c) {
        const float e0 = blo(wq[i][c]), e1 = bhi(wq[i][c]);
        a[2 * c] = fmaf(e0, xa, a[2 * c]);
        a[2 * c + 1] = fmaf(e1, xa, a[2 * c + 1]);
        a[8 + 2 * c] = fmaf(e0, xb, a[8 + 2 * c]);
        a[8 + 2 * c + 1] = fmaf(e1, xb, a[8 + 2 * c + 1]);
      }
    }
#pragma unroll
    for (int i = 0; i < 8; ++i) a[i] = add32(a[i], a[i + 8]);
#pragma unroll
    for (int i = 0; i < 4; ++i) a[i] = add16(a[i], a[i + 4]);
    sm.qred[w][l] = f32x4{a[0], a[1], a[2], a[3]};
  }
  asm volatile("s_waitcnt lgkmcnt(0)" : "+s"(ep)::"memory");
  const unsigned tag = ep + 0x01010101u;
  const u32x4 tf = {tag & 0x3ffu, (tag >> 8) & 0x3ffu, (tag >> 16) & 0x3ffu, ((tag >> 24) | (tag << 8)) & 0x3ffu}, tq = tf & 0xffu;

  bar();
  if (w < 2) {
    f32x2 t[8];
#pragma unroll
    for (int i = 0; i < 8; ++i) t[i] = ((const f32x2*)&sm.qred[i][0])[tid];
    const f32x2 r = ((t[0] + t[1]) + (t[2] + t[3])) + ((t[4] + t[5]) + (t[6] + t[7]));
    const unsigned e0 = enc8(r[0], (l & 1) ? tq[2] : tq[0]), e1 = enc8(r[1], (l & 1) ? tq[3] : tq[1]);
    st_sc1(u32x4{e0, e1, dppu<QP(1, 0, 3, 2)>(e0), dppu<QP(1, 0, 3, 2)>(e1)}, rws, (l & 1) ? DROP : (l & 30) * 16 + (l >> 5) * 16,
           WS_Q + b * 131072 + w * 65536 + kvh * 8192 + s * 512);
  }
  SB;
  LDK(0, 8)
  LDK(8, 16)
  const bool odd = l & 1;
  u32x4 tv = tf;
  unsigned hi22 = ~0x3ffu, f0 = odd ? tf[2] : tf[0], f1 = odd ? tf[3] : tf[1];
  unsigned voh1 = (odd || w >= 4) ? DROP : (l >> 4) * 16384 + (l & 15) * 8, sh1 = WS_H1 + kvh * 65536 + w * 4096 + j * 128, sh1b = sh1 + 524288;
  unsigned soml = WS_ML + bc * 16384 + kc * 2048 + (s >> 2) * 512 + ((w >> 1) & 1) * 256;
  unsigned voml = (((l & 15) + (l >> 4) + 4 * (w & 1)) & 15) * 16;
  unsigned soh = WS_H1 + bc * 524288 + kc * 65536 + s * 4096 + ((w >> 1) & 1) * 2048 + (w & 1) * 512;
  unsigned voh = ((l >> 4) + (l & 8)) * 128 + (l & 7) * 16;
  LDS float* po2w = (LDS float*)&sm.o2[(w >> 1) & 1][(8 * (l >> 4) + 4 * (l & 1) + ((l >> 1) & 3)) * 2 + (w & 1)];
  const LDS f32x2* po2r[4];
#pragma unroll
  for (int i = 0; i < 4; ++i) po2r[i] = (const LDS f32x2*)&sm.o2[(l >> 2) & 1][(((i + s) & 3) * 8 + (l >> 4) * 2 + ((l >> 3) & 1)) * 2];
  unsigned vh2 = (l & 8) ? DROP : ((l >> 4) * 4 + ((l >> 1) & 3)) * 4096 + (l & 1) * 16;
  unsigned sh2 = WS_H2 + bc * 524288 + w * 65536 + (kc * 16 + s) * 32, sg2 = WS_H2 + (bc ^ 1) * 524288 + ci * 4096 + (w & 3) * 1024;
  LDS float* po3w = (LDS float*)&sm.o3[(l >> 4) * 2 + (l & 1)] + (w & 3);
  unsigned vout = l >= 8 ? DROP : (((ci >> 1) & 1) * 1024 + (bc ^ 1) * 512 + 64 * (ci >> 4) + 32 * ((ci >> 2) & 1) + 8 * (l >> 1) + 4 * ((ci >> 3) & 1) + 2 * (ci & 1) + (l & 1)) * 2;
  {
    const unsigned soq = WS_Q + (w >> 2) * 131072 + b * 65536 + kvh * 8192 + (w & 3) * 2048;
    u32x4 g0 = ld_sc1(rws, l * 16, soq);
    SB;
    u32x4 g1 = ld_sc1(rws, l * 16 + 1024, soq);
    SB;
    LDV(0, 4)
    asm volatile("" : "+v"(f0), "+v"(f1), "+v"(voh1), "+v"(voml), "+v"(voh), "+v"(vh2), "+v"(vout), "+v"(po2w), "+v"(po3w));
    asm volatile("" : "+v"(po2r[0]), "+v"(po2r[1]), "+v"(po2r[2]), "+v"(po2r[3]), "+v"(tv));
    asm volatile("" : "+s"(hi22), "+s"(sh1), "+s"(sh1b), "+s"(soml), "+s"(soh), "+s"(sh2), "+s"(sg2));
    SB;
#define QBAD (((g0[0] | g0[1] | g0[2] | g0[3] | g1[0] | g1[1] | g1[2] | g1[3]) & 0xffu) != 0)
    g0 ^= tq; g1 ^= tq;
    if (__any(QBAD)) {
      for (int it = 0; it < SPIN_CAP; ++it) {
        CLOB;
        g0 = ld_sc1(rws, l * 16, soq) ^ tq;
        g1 = ld_sc1(rws, l * 16 + 1024, soq) ^ tq;
        if (!__any(QBAD)) break;
      }
    }
    const f32x4 x = __builtin_bit_cast(f32x4, g0) + __builtin_bit_cast(f32x4, g1);
    sm.qs[w >> 2][w & 3][2 * (l & 31) + (l >> 5)] = pk(add32(x[0], x[2]), add32(x[1], x[3]));
  }
  SB;
  bar();
  LDV(4, 12)

  const int h = (l >> 2) & 3;
  const unsigned m0 = (l & 2) ? 0u : (odd ? 0xffff0000u : 0xffffu), m1 = (l & 2) ? (odd ? 0xffff0000u : 0xffffu) : 0u;
  const unsigned t0 = (l & 2) ? 0u : ~0u;
  u32x4 qf[4];
  {
    const u32x4 q0 = *(const u32x4*)&sm.qs[0][h][(l >> 4) * 8 + (l & 1) * 4], q1 = *(const u32x4*)&sm.qs[0][h][32 + (l >> 4) * 8 + (l & 1) * 4];
    qf[0] = q0 & t0; qf[1] = q1 & t0; qf[2] = q0 & ~t0; qf[3] = q1 & ~t0;
    const float u = qk_group(ka, qf, odd);
    const float m2 = head_max(u) * LOG2E;
    const float p = ex2(fmaf(u, LOG2E, -m2));
    const float ls = head_sum(p);
    f32x4 lo = {0.f, 0.f, 0.f, 0.f}, hi = lo;
    pv_group(va, pk(p, p), m0, m1, lo, hi);
    sm.acc[0][w][2 * l] = lo; sm.acc[0][w][2 * l + 1] = hi;
    if ((l & 0x33) == 0) sm.ml[0][h][w] = f32x2{m2, ls};
  }
  SB;
  LDV(12, 16)
  unsigned P[4];
  float m2B;
  {
    const u32x4 q0 = *(const u32x4*)&sm.qs[1][h][(l >> 4) * 8 + (l & 1) * 4], q1 = *(const u32x4*)&sm.qs[1][h][32 + (l >> 4) * 8 + (l & 1) * 4];
    qf[0] = q0 & t0; qf[1] = q1 & t0; qf[2] = q0 & ~t0; qf[3] = q1 & ~t0;
    float u[4];
    u[0] = qk_group(kb, qf, odd);
    u[1] = qk_group(kb + 4, qf, odd);
    u[2] = qk_group(kb + 8, qf, odd);
    u[3] = qk_group(kb + 12, qf, odd);
    m2B = head_max(fmaxf(fmaxf(u[0], u[1]), fmaxf(u[2], u[3]))) * LOG2E;
    float ls = 0.f;
#pragma unroll
    for (int i = 0; i < 4; ++i) { const float p = ex2(fmaf(u[i], LOG2E, -m2B)); ls += p; P[i] = pk(p, p); }
    ls = head_sum(ls);
    if ((l & 0x33) == 0) sm.ml[1][h][w] = f32x2{m2B, ls};
  }
  bar();
  const unsigned vw = (l >> 3) * 1024 + (l & 7) * 16;
#pragma unroll
  for (int i = 0; i < 4; ++i) { wo[i] = ld_nt(rwo, vw, ((i + s) & 3) * 8192 + w * 128); SB; }
  float fB;
  {
    const f32x4* mp = (const f32x4*)&sm.ml[1][h][0];
    const f32x4 a0 = mp[0], a1 = mp[1], a2 = mp[2], a3 = mp[3];
    const float M = fmaxf(fmaxf(fmaxf(a0[0], a0[2]), fmaxf(a1[0], a1[2])), fmaxf(fmaxf(a2[0], a2[2]), fmaxf(a3[0], a3[2])));
    fB = ex2(m2B - M);
  }
  if (w < 4) {
    const int hh = (l >> 4) & 3;
    float mw[8], M = -INFINITY;
#pragma unroll
    for (int i = 0; i < 8; ++i) { mw[i] = sm.ml[0][hh][i][0]; M = fmaxf(M, mw[i]); }
    f32x2 t = {0.f, 0.f};
#pragma unroll
    for (int i = 0; i < 8; ++i) t += ((const f32x2*)&sm.acc[0][i][0])[tid] * ex2(mw[i] - M);
    const unsigned e0 = enc(t[0], f0, hi22), e1 = enc(t[1], f1, hi22);
    const u32x4 v = {e0, e1, dppu<QP(1, 0, 3, 2)>(e0), dppu<QP(1, 0, 3, 2)>(e1)};
    st_sc1(v, rws, voh1, sh1);
  } else if (w == 4) {
    const f32x2 v = sm.ml[(l >> 5) & 1][(l >> 3) & 3][l & 7];
    float M = v[0];
    M = fmaxf(M, dpp<QP(1, 0, 3, 2)>(M));
    M = fmaxf(M, dpp<QP(2, 3, 0, 1)>(M));
    M = fmaxf(M, dpp<0x141>(M));
    float L = v[1] * ex2(v[0] - M);
    L += dpp<QP(1, 0, 3, 2)>(L);
    L += dpp<QP(2, 3, 0, 1)>(L);
    L += dpp<0x141>(L);
    if ((l & 7) == 0) st_sc1(u32x4{asu(M), tag, asu(L), tag}, rws, (l >> 5) * 16384 + ((l >> 3) & 3) * 512, WS_ML + kvh * 2048 + j * 16);
  }
  SB;
  {
    f32x4 lo = {0.f, 0.f, 0.f, 0.f}, hi = lo;
    pv_group(vb, P[0], m0, m1, lo, hi);
#pragma unroll
    for (int G = 1; G < 4; ++G) pv_group(vb + 4 * G, P[G], m0, m1, lo, hi);
    sm.acc[1][w][2 * l] = lo * fB; sm.acc[1][w][2 * l + 1] = hi * fB;
  }
  u32x4 gm, gd, wa[4];
  bar();
#define WPIN do { _Pragma("unroll") for (int i = 0; i < 2; ++i) { \
      wa[2 * i] = u32x4{wo[2 * i][0], wo[2 * i][1], wo[2 * i + 1][0], wo[2 * i + 1][1]}; \
      wa[2 * i + 1] = u32x4{wo[2 * i][2], wo[2 * i][3], wo[2 * i + 1][2], wo[2 * i + 1][3]}; } \
    asm volatile("" : "+v"(wa[0]), "+v"(wa[1]), "+v"(wa[2]), "+v"(wa[3])); } while (0)
  {
    u32x4 v = tv;
    if (w < 4) {
      f32x2 t[8];
#pragma unroll
      for (int i = 0; i < 8; ++i) t[i] = ((const f32x2*)&sm.acc[1][i][0])[tid];
      const f32x2 r = ((t[0] + t[1]) + (t[2] + t[3])) + ((t[4] + t[5]) + (t[6] + t[7]));
      const unsigned e0 = enc(r[0], f0, hi22), e1 = enc(r[1], f1, hi22);
      v = u32x4{e0, e1, dppu<QP(1, 0, 3, 2)>(e0), dppu<QP(1, 0, 3, 2)>(e1)};
    }
    st_sc1(v, rws, voh1, sh1b);
  }
  SB;
  if (w < 4) {
    WPIN;
  } else {
    gm = ld_sc1(rws, voml, soml);
    gd = ld_sc1(rws, voh, soh);
    SB;
    for (int it = 0; (gm[1] != tag || gm[3] != tag) && it < SPIN_CAP; ++it) {
      CLOB;
      gm = ld_sc1(rws, voml, soml);
    }
    const float mm = asf(gm[0]);
    float M = fmaxf(mm, dpp<ROR(1)>(mm));
    M = fmaxf(M, dpp<ROR(2)>(M));
    M = fmaxf(M, dpp<ROR(4)>(M));
    M = fmaxf(M, dpp<ROR(8)>(M));
    const float e = ex2(mm - M);
    float L = e * asf(gm[2]);
    L += dpp<ROR(1)>(L);
    L += dpp<ROR(2)>(L);
    L += dpp<ROR(4)>(L);
    L += dpp<ROR(8)>(L);
    const float el = e / L;
    const float wgt = asf((unsigned)__builtin_amdgcn_update_dpp((int)asu(dpp<NBC(0)>(el)), (int)asu(el), NBC(8), 0xf, 0xc, false));
    WPIN;
    SB;
    const f32x4 x = poll4(gd, tv, rws, voh, soh) * wgt;
    float v = add16(add32(x[0], x[2]), add32(x[1], x[3]));
    v += dpp<ROR(8)>(v);
    *po2w = v;
  }
  bar();
  {
    f32x2 t[4];
#pragma unroll
    for (int i = 0; i < 4; ++i) t[i] = *po2r[i];
    SB;
    unsigned x[8];
#pragma unroll
    for (int i = 0; i < 4; ++i) {
      const float o = t[i][0] + t[i][1];
      const unsigned pb = pk(o, o);
      x[2 * i] = pb & m0; x[2 * i + 1] = pb & m1;
    }
    f32x4 lo = {0.f, 0.f, 0.f, 0.f}, hi = lo;
    lo = mfma32(wa[0], u32x4{x[0], x[1], x[2], x[3]}, lo);
    hi = mfma32(wa[1], u32x4{x[0], x[1], x[2], x[3]}, hi);
    lo = mfma32(wa[2], u32x4{x[4], x[5], x[6], x[7]}, lo);
    hi = mfma32(wa[3], u32x4{x[4], x[5], x[6], x[7]}, hi);
    u32x4 r;
#pragma unroll
    for (int i = 0; i < 4; ++i) {
      const u32x2 t2 = sw32(lo[i], hi[i]);
      r[i] = enc(asf(t2[0]) + dpp<ROR(8)>(asf(t2[1])), tv[i], hi22);
    }
    st_sc1(r, rws, vh2, sh2);
  }
  SB;
  if (w < 4) {
    const f32x4 x = poll4(ld_sc1(rws, vov, sg2), tv, rws, vov, sg2);
    float v = add16(add32(x[0], x[2]), add32(x[1], x[3]));
    v += dpp<ROR(8)>(v);
    v += dpp<ROR(4)>(v);
    v += dpp<ROR(2)>(v);
    *po3w = v;
  }
  bar();
  if (w == 0) {
    const f32x4 a = sm.o3[l & 7];
    const float r = (a[0] + a[1]) + (a[2] + a[3]);
    __builtin_amdgcn_raw_buffer_store_b16((u16)pk(r, r), mk(out, 4096), vout, 0, 0);
    if (blk == 0) __builtin_amdgcn_raw_buffer_store_b32(tag, rws, l ? DROP : 0u, WS_EPOCH, AUX_SC1);
  }
}

extern "C" void kernel_launch(void* const* d_in, const int* in_sizes, int n_in, void* d_out, int out_size, void* d_ws,
                              size_t ws_size, hipStream_t stream) {
  fused_decode<<<256, 512, 0, stream>>>(KARGS(d_in, d_out, d_ws));
}
```

```cpp
#include <hip/hip_runtime.h>
#include <hip/hip_bf16.h>
#include <stdint.h>

typedef unsigned char u8;
typedef unsigned short u16;
typedef __attribute__((ext_vector_type(4))) unsigned u32x4;
typedef __attribute__((ext_vector_type(2))) unsigned u32x2;
typedef __attribute__((ext_vector_type(4))) float f32x4;
typedef __attribute__((ext_vector_type(2))) float f32x2;
typedef __attribute__((ext_vector_type(8))) __bf16 bf16x8;
typedef __attribute__((ext_vector_type(2))) __bf16 bf16x2;
typedef __attribute__((ext_vector_type(4))) short s16x4;
typedef __amdgpu_buffer_rsrc_t rsrc_t;
#define LDS __attribute__((address_space(3)))

#define SB __builtin_amdgcn_sched_barrier(0)
#define CLOB asm volatile("" ::: "memory")
#define DEVI __device__ __forceinline__

#define WS_EPOCH 0u
#define WS_Q     4096u
#define WS_ML    (WS_Q + 524288u)
#define WS_H1    (WS_ML + 32768u)
#define WS_H2    (WS_H1 + 1048576u)
#define WS_END   (WS_H2 + 1048576u)
#define SPIN_CAP 100000

#define DROP 0x80000000u
#define AUX_NT  2
#define AUX_SC1 16

DEVI rsrc_t mk(const void* p, unsigned bytes) {
  return __builtin_amdgcn_make_buffer_rsrc((void*)p, (short)0, (int)bytes, 0x00020000);
}
DEVI u32x4 ld_nt(rsrc_t r, unsigned voff, unsigned soff) {
  return __builtin_amdgcn_raw_buffer_load_b128(r, (int)voff, (int)soff, AUX_NT);
}
DEVI u32x4 ld_sc1(rsrc_t r, unsigned voff, unsigned soff) {
  return __builtin_amdgcn_raw_buffer_load_b128(r, (int)voff, (int)soff, AUX_SC1);
}
DEVI void st_sc1(u32x4 v, rsrc_t r, unsigned voff, unsigned soff) {
  __builtin_amdgcn_raw_buffer_store_b128(v, r, (int)voff, (int)soff, AUX_SC1);
}
DEVI float asf(unsigned v) { return __uint_as_float(v); }
DEVI unsigned asu(float v) { return __float_as_uint(v); }
DEVI float blo(unsigned v) { return asf(v << 16); }
DEVI float bhi(unsigned v) { return asf(v & 0xffff0000u); }
DEVI unsigned pk(float a, float b) {
  f32x2 f = {a, b};
  bf16x2 h = __builtin_convertvector(f, bf16x2);
  return __builtin_bit_cast(unsigned, h);
}
template <int CTRL> DEVI float dpp(float v) {
  return asf((unsigned)__builtin_amdgcn_update_dpp(0, (int)asu(v), CTRL, 0xf, 0xf, true));
}
template <int CTRL> DEVI unsigned dppu(unsigned v) {
  return (unsigned)__builtin_amdgcn_update_dpp(0, (int)v, CTRL, 0xf, 0xf, true);
}
#define QP(a, b, c, d) ((a) | ((b) << 2) | ((c) << 4) | ((d) << 6))
#define ROR(n) (0x120 + (n))
#define NBC(n) (0x150 + (n))
DEVI u32x2 sw32(float a, float b) { auto r = __builtin_amdgcn_permlane32_swap(asu(a), asu(b), false, false); return u32x2{r[0], r[1]}; }
DEVI u32x2 sw16(float a, float b) { auto r = __builtin_amdgcn_permlane16_swap(asu(a), asu(b), false, false); return u32x2{r[0], r[1]}; }
DEVI float add32(float a, float b) { u32x2 r = sw32(a, b); return asf(r[0]) + asf(r[1]); }
DEVI float add16(float a, float b) { u32x2 r = sw16(a, b); return asf(r[0]) + asf(r[1]); }
DEVI float max32(float a) { u32x2 r = sw32(a, a); return fmaxf(asf(r[0]), asf(r[1])); }
DEVI float max16(float a) { u32x2 r = sw16(a, a); return fmaxf(asf(r[0]), asf(r[1])); }
DEVI float ex2(float x) { return __builtin_amdgcn_exp2f(x); }
DEVI f32x4 mfma32(u32x4 a, u32x4 b, f32x4 c) {
  return __builtin_amdgcn_mfma_f32_16x16x32_bf16(__builtin_bit_cast(bf16x8, a), __builtin_bit_cast(bf16x8, b), c, 0, 0, 0);
}
DEVI f32x4 mfma16(unsigned a0, unsigned a1, unsigned b0, unsigned b1, f32x4 c) {
  u32x2 a = {a0, a1}, b = {b0, b1};
  return __builtin_amdgcn_mfma_f32_16x16x16bf16_1k(__builtin_bit_cast(s16x4, a), __builtin_bit_cast(s16x4, b), c, 0, 0, 0);
}
DEVI void bar() { __builtin_amdgcn_fence(__ATOMIC_RELEASE, "workgroup", "local"); __builtin_amdgcn_s_barrier(); __builtin_amdgcn_fence(__ATOMIC_ACQUIRE, "workgroup", "local"); }

DEVI float qk_group(const u32x4* k, const u32x4* qf, bool odd) {
  f32x4 d = {0.f, 0.f, 0.f, 0.f};
  d = mfma32(k[0], qf[0], d);
  d = mfma32(k[1], qf[1], d);
  d = mfma32(k[2], qf[2], d);
  d = mfma32(k[3], qf[3], d);
  float a = odd ? d[3] : d[0], b = odd ? d[1] : d[2];
  float u = a + dpp<QP(1, 0, 3, 2)>(b);
  u = blo(pk(u, u)) * 0.08838834764831845f;
  return blo(pk(u, u));
}
DEVI float head_max(float m) {
  m = fmaxf(m, dpp<QP(1, 0, 3, 2)>(m));
  m = fmaxf(m, dpp<QP(2, 3, 0, 1)>(m));
  return max32(max16(m));
}
DEVI float head_sum(float m) {
  m += dpp<QP(1, 0, 3, 2)>(m);
  m += dpp<QP(2, 3, 0, 1)>(m);
  m = add16(m, m);
  return add32(m, m);
}
template <int I> DEVI void pv_one(u32x4 v, unsigned P, unsigned m0, unsigned m1, f32x4& lo, f32x4& hi) {
  unsigned pb = dppu<QP(I, I, I, I)>(P);
  unsigned x0 = pb & m0, x1 = pb & m1;
  lo = mfma16(v[0], v[1], x0, x1, lo);
  hi = mfma16(v[2], v[3], x0, x1, hi);
}
DEVI void pv_group(const u32x4* v, unsigned P, unsigned m0, unsigned m1, f32x4& lo, f32x4& hi) {
  pv_one<0>(v[0], P, m0, m1, lo, hi);
  pv_one<1>(v[1], P, m0, m1, lo, hi);
  pv_one<2>(v[2], P, m0, m1, lo, hi);
  pv_one<3>(v[3], P, m0, m1, lo, hi);
}

DEVI unsigned enc(float v, unsigned f, unsigned hi) { return __builtin_amdgcn_bitop3_b32(asu(v) + 0x200u, hi, f, 0xea); }
DEVI unsigned enc8(float v, unsigned f) { return ((asu(v) + 0x80u) & ~0xffu) | f; }
DEVI f32x4 poll4(u32x4 g, u32x4 f, rsrc_t r, unsigned voff, unsigned soff) {
  u32x4 x = g ^ f;
  for (int it = 0; ((x[0] | x[1] | x[2] | x[3]) & 0x3ffu) != 0 && it < SPIN_CAP; ++it) {
    CLOB;
    x = ld_sc1(r, voff, soff) ^ f;
  }
  return __builtin_bit_cast(f32x4, x);
}

#define KARGS(d, o, w) (const u8*)d[3], (const u8*)d[8], (const u8*)d[0], (const u8*)d[5], (u8*)w, (const u8*)d[1], (const u8*)d[2], \
                       (const u8*)d[6], (const u8*)d[7], (const u8*)d[4], (const u8*)d[9], (u8*)o
struct Smem {
  f32x4 qred[8][64];
  unsigned qs[2][4][64];
  f32x4 acc[2][8][128];
  f32x2 ml[2][4][8];
  float o2[2][80];
  f32x4 o3[8];
};

__global__ void __launch_bounds__(512, 2)
fused_decode(const u8* __restrict__ wqA, const u8* __restrict__ wqB, const u8* __restrict__ xA, const u8* __restrict__ xB, u8* ws,
             const u8* __restrict__ kA, const u8* __restrict__ vA, const u8* __restrict__ kB, const u8* __restrict__ vB,
             const u8* __restrict__ woA, const u8* __restrict__ woB, u8* __restrict__ out) {
  __shared__ Smem sm;
  const int tid = threadIdx.x, l = tid & 63, w = __builtin_amdgcn_readfirstlane(tid >> 6);
  const int blk = blockIdx.x, kvh = blk & 7, j = blk >> 3,
            b = j >> 4, s = j & 15, g = b * 8 + kvh;
  const float LOG2E = 1.4426950408889634f;
  const int kq = s >> 1, hq = ((s & 1) << 1) | ((kvh & 1) ^ 1), qq = kvh >> 1;
  unsigned vo = (l >> 4) * 32768 + (l & 15) * 16, so = qq * 1048576 + w * 131072 + kq * 1024 + hq * 256;
  unsigned vx = (l >> 4) * 8, sx = qq * 256 + w * 32, so1 = so + 8192, so2 = so + 16384, so3 = so + 24576;
  asm volatile("" : "+v"(vo), "+v"(vx), "+s"(so), "+s"(so1), "+s"(so2), "+s"(so3), "+s"(sx));
  SB;
  asm volatile("" :: "s"(wqA), "s"(wqB), "s"(xA), "s"(xB), "s"(ws), "s"(kA), "s"(vA), "s"(kB), "s"(vB), "s"(woA), "s"(woB), "s"(out));
  const rsrc_t rwq = mk(b ? wqB : wqA, 4194304);
  const rsrc_t rx = mk(b ? xB : xA, 2048);
  u32x4 wq[4], ka[4], va[4], kb[16], vb[16], wo[4];
  u32x2 x0, x1;
  x0 = __builtin_amdgcn_raw_buffer_load_b64(rx, vx, sx, 0);
  x1 = __builtin_amdgcn_raw_buffer_load_b64(rx, vx + 1024, sx, 0);
  SB;
  wq[0] = ld_nt(rwq, vo, so); SB;
  wq[1] = ld_nt(rwq, vo, so1); SB;
  wq[2] = ld_nt(rwq, vo, so2); SB;
  wq[3] = ld_nt(rwq, vo, so3); SB;
  unsigned ep;
  asm volatile("s_load_dword %0, %1, 0x0" : "=&s"(ep) : "s"(ws) : "memory");
  SB;
  const int bc = kvh >> 2, kc = ((kvh & 4) ^ (b * 4)) | ((kvh + 1 - b) & 3), ci = (kvh & 3) * 32 + j;
  const rsrc_t rws = mk(ws, WS_END);
  const rsrc_t rwo = mk((bc ? woB : woA) + (size_t)(kc * 512 + s * 32) * 1024, 32768);
  const rsrc_t rkA = mk(kA + ((size_t)g * 8192 + 6144) * 256, 524288);
  const rsrc_t rvA = mk(vA + ((size_t)g * 8192 + 6144) * 256, 524288);
  const rsrc_t rkB = mk(kB + (size_t)g * 8192 * 256, 2097152);
  const rsrc_t rvB = mk(vB + (size_t)g * 8192 * 256, 2097152);
  const unsigned vok = (4 * ((l >> 1) & 1) + ((l >> 2) & 3)) * 256 + (l >> 4) * 32 + (l & 1) * 16, vov = l * 16;
#define SOA(t) ((unsigned)((s * 16 + w * 2 + (t) + 17 * g) & 255) * 2048u)
#define SOB(t) ((unsigned)(((((w >> 2) * 2 + ((t) >> 2) + s) & 3) * 256) + ((s * 16 + (w & 3) * 4 + ((t) & 3) + 65 * g) & 255)) * 2048u)
  __builtin_amdgcn_s_barrier();
  SB;
#pragma unroll
  for (int i = 0; i < 4; ++i) { ka[i] = ld_nt(rkA, vok + (i & 1) * 128, SOA(i >> 1)); SB; }
#pragma unroll
  for (int i = 0; i < 4; ++i) { va[i] = ld_nt(rvA, vov + (i & 1) * 1024, SOA(i >> 1)); SB; }
#define LDK(a, e) _Pragma("unroll") for (int i = a; i < e; ++i) { kb[i] = ld_nt(rkB, vok + (i & 1) * 128, SOB(i >> 1)); SB; }
#define LDV(a, e) _Pragma("unroll") for (int i = a; i < e; ++i) { vb[i] = ld_nt(rvB, vov + (i & 1) * 1024, SOB(i >> 1)); SB; }
  LDK(0, 2)
  {
    float a[16];
#pragma unroll
    for (int i = 0; i < 16; ++i) a[i] = 0.f;
#pragma unroll
    for (int i = 0; i < 4; ++i) {
      const float xa = (i & 1) ? bhi(x0[i >> 1]) : blo(x0[i >> 1]), xb = (i & 1) ? bhi(x1[i >> 1]) : blo(x1[i >> 1]);
#pragma unroll
      for (int c = 0; c < 4; ++c) {
        const float e0 = blo(wq[i][c]), e1 = bhi(wq[i][c]);
        a[2 * c] = fmaf(e0, xa, a[2 * c]);
        a[2 * c + 1] = fmaf(e1, xa, a[2 * c + 1]);
        a[8 + 2 * c] = fmaf(e0, xb, a[8 + 2 * c]);
        a[8 + 2 * c + 1] = fmaf(e1, xb, a[8 + 2 * c + 1]);
      }
    }
#pragma unroll
    for (int i = 0; i < 8; ++i) a[i] = add32(a[i], a[i + 8]);
#pragma unroll
    for (int i = 0; i < 4; ++i) a[i] = add16(a[i], a[i + 4]);
    sm.qred[w][l] = f32x4{a[0], a[1], a[2], a[3]};
  }
  asm volatile("s_waitcnt lgkmcnt(0)" : "+s"(ep)::"memory");
  const unsigned tag = ep + 0x01010101u;
  const u32x4 tf = {tag & 0x3ffu, (tag >> 8) & 0x3ffu, (tag >> 16) & 0x3ffu, ((tag >> 24) | (tag << 8)) & 0x3ffu}, tq = tf & 0xffu;

  bar();
  if (w < 2) {
    f32x2 t[8];
#pragma unroll
    for (int i = 0; i < 8; ++i) t[i] = ((const f32x2*)&sm.qred[i][0])[tid];
    const f32x2 r = ((t[0] + t[1]) + (t[2] + t[3])) + ((t[4] + t[5]) + (t[6] + t[7]));
    const unsigned e0 = enc8(r[0], (l & 1) ? tq[2] : tq[0]), e1 = enc8(r[1], (l & 1) ? tq[3] : tq[1]);
    st_sc1(u32x4{e0, e1, dppu<QP(1, 0, 3, 2)>(e0), dppu<QP(1, 0, 3, 2)>(e1)}, rws, (l & 1) ? DROP : (l & 30) * 16 + (l >> 5) * 16,
           WS_Q + b * 131072 + w * 65536 + kq * 8192 + (hq * 4 + qq) * 512);
  }
  SB;
  LDK(2, 16)
  const bool odd = l & 1;
  u32x4 tv = tf;
  unsigned hi22 = ~0x3ffu, f0 = odd ? tf[2] : tf[0], f1 = odd ? tf[3] : tf[1];
  unsigned voh1 = (odd || w >= 4) ? DROP : (l >> 4) * 16384 + (l & 15) * 8, sh1 = WS_H1 + kvh * 65536 + w * 4096 + j * 128, sh1b = sh1 + 524288;
  unsigned soml = WS_ML + bc * 16384 + kc * 2048 + (s >> 2) * 512 + ((w >> 1) & 1) * 256;
  unsigned voml = (((l & 15) + (l >> 4) + 4 * (w & 1)) & 15) * 16;
  unsigned soh = WS_H1 + bc * 524288 + kc * 65536 + s * 4096 + ((w >> 1) & 1) * 2048 + (w & 1) * 512;
  unsigned voh = ((l >> 4) + (l & 8)) * 128 + (l & 7) * 16;
  LDS float* po2w = (LDS float*)&sm.o2[(w >> 1) & 1][(8 * (l >> 4) + 4 * (l & 1) + ((l >> 1) & 3)) * 2 + (w & 1)];
  const LDS f32x2* po2r[4];
#pragma unroll
  for (int i = 0; i < 4; ++i) po2r[i] = (const LDS f32x2*)&sm.o2[(l >> 2) & 1][(((i + s) & 3) * 8 + (l >> 4) * 2 + ((l >> 3) & 1)) * 2];
  unsigned vh2 = (l & 8) ? DROP : ((l >> 4) * 4 + ((l >> 1) & 3)) * 4096 + (l & 1) * 16;
  unsigned sh2 = WS_H2 + bc * 524288 + w * 65536 + (kc * 16 + s) * 32, sg2 = WS_H2 + (bc ^ 1) * 524288 + ci * 4096 + (w & 3) * 1024;
  LDS float* po3w = (LDS float*)&sm.o3[(l >> 4) * 2 + (l & 1)] + (w & 3);
  unsigned vout = l >= 8 ? DROP : (((ci >> 1) & 1) * 1024 + (bc ^ 1) * 512 + 64 * (ci >> 4) + 32 * ((ci >> 2) & 1) + 8 * (l >> 1) + 4 * ((ci >> 3) & 1) + 2 * (ci & 1) + (l & 1)) * 2;
  unsigned tc0, tc1, tc2;
  {
    const unsigned soq = WS_Q + (w >> 2) * 131072 + b * 65536 + kvh * 8192 + (w & 3) * 2048;
    u32x4 g0 = ld_sc1(rws, l * 16, soq);
    SB;
    u32x4 g1 = ld_sc1(rws, l * 16 + 1024, soq);
    SB;
    tc0 = __builtin_amdgcn_raw_buffer_load_b32(rws, w == 7 ? l * 65536 : DROP, 0, AUX_SC1);
    tc2 = __builtin_amdgcn_raw_buffer_load_b32(mk(out, 4096), w == 7 ? l * 4096 : DROP, 0, AUX_SC1);
    SB;
    LDV(0, 4)
    asm volatile("" : "+v"(f0), "+v"(f1), "+v"(voh1), "+v"(voml), "+v"(voh), "+v"(vh2), "+v"(vout), "+v"(po2w), "+v"(po3w));
    asm volatile("" : "+v"(po2r[0]), "+v"(po2r[1]), "+v"(po2r[2]), "+v"(po2r[3]), "+v"(tv));
    asm volatile("" : "+s"(hi22), "+s"(sh1), "+s"(sh1b), "+s"(soml), "+s"(soh), "+s"(sh2), "+s"(sg2));
    SB;
#define QBAD (((g0[0] | g0[1] | g0[2] | g0[3] | g1[0] | g1[1] | g1[2] | g1[3]) & 0xffu) != 0)
    g0 ^= tq; g1 ^= tq;
    if (__any(QBAD)) {
      for (int it = 0; it < SPIN_CAP; ++it) {
        CLOB;
        g0 = ld_sc1(rws, l * 16, soq) ^ tq;
        g1 = ld_sc1(rws, l * 16 + 1024, soq) ^ tq;
        if (!__any(QBAD)) break;
      }
    }
    const f32x4 x = __builtin_bit_cast(f32x4, g0) + __builtin_bit_cast(f32x4, g1);
    sm.qs[w >> 2][w & 3][2 * (l & 31) + (l >> 5)] = pk(add32(x[0], x[2]), add32(x[1], x[3]));
  }
  SB;
  bar();
  LDV(4, 12)

  const int h = (l >> 2) & 3;
  const unsigned m0 = (l & 2) ? 0u : (odd ? 0xffff0000u : 0xffffu), m1 = (l & 2) ? (odd ? 0xffff0000u : 0xffffu) : 0u;
  const unsigned t0 = (l & 2) ? 0u : ~0u;
  u32x4 qf[4];
  {
    const u32x4 q0 = *(const u32x4*)&sm.qs[0][h][(l >> 4) * 8 + (l & 1) * 4], q1 = *(const u32x4*)&sm.qs[0][h][32 + (l >> 4) * 8 + (l & 1) * 4];
    qf[0] = q0 & t0; qf[1] = q1 & t0; qf[2] = q0 & ~t0; qf[3] = q1 & ~t0;
    const float u = qk_group(ka, qf, odd);
    const float m2 = head_max(u) * LOG2E;
    const float p = ex2(fmaf(u, LOG2E, -m2));
    const float ls = head_sum(p);
    f32x4 lo = {0.f, 0.f, 0.f, 0.f}, hi = lo;
    pv_group(va, pk(p, p), m0, m1, lo, hi);
    sm.acc[0][w][2 * l] = lo; sm.acc[0][w][2 * l + 1] = hi;
    if ((l & 0x33) == 0) sm.ml[0][h][w] = f32x2{m2, ls};
  }
  SB;
  LDV(12, 16)
  unsigned P[4];
  float m2B;
  {
    const u32x4 q0 = *(const u32x4*)&sm.qs[1][h][(l >> 4) * 8 + (l & 1) * 4], q1 = *(const u32x4*)&sm.qs[1][h][32 + (l >> 4) * 8 + (l & 1) * 4];
    qf[0] = q0 & t0; qf[1] = q1 & t0; qf[2] = q0 & ~t0; qf[3] = q1 & ~t0;
    float u[4];
    u[0] = qk_group(kb, qf, odd);
    u[1] = qk_group(kb + 4, qf, odd);
    u[2] = qk_group(kb + 8, qf, odd);
    u[3] = qk_group(kb + 12, qf, odd);
    m2B = head_max(fmaxf(fmaxf(u[0], u[1]), fmaxf(u[2], u[3]))) * LOG2E;
    float ls = 0.f;
#pragma unroll
    for (int i = 0; i < 4; ++i) { const float p = ex2(fmaf(u[i], LOG2E, -m2B)); ls += p; P[i] = pk(p, p); }
    ls = head_sum(ls);
    if ((l & 0x33) == 0) sm.ml[1][h][w] = f32x2{m2B, ls};
  }
  bar();
  const unsigned vw = (l >> 3) * 1024 + (l & 7) * 16;
#pragma unroll
  for (int i = 0; i < 4; ++i) { wo[i] = ld_nt(rwo, vw, ((i + s) & 3) * 8192 + w * 128); SB; }
  float fB;
  {
    const f32x4* mp = (const f32x4*)&sm.ml[1][h][0];
    const f32x4 a0 = mp[0], a1 = mp[1], a2 = mp[2], a3 = mp[3];
    const float M = fmaxf(fmaxf(fmaxf(a0[0], a0[2]), fmaxf(a1[0], a1[2])), fmaxf(fmaxf(a2[0], a2[2]), fmaxf(a3[0], a3[2])));
    fB = ex2(m2B - M);
  }
  u32x4 sv = tv;
  unsigned svo = voh1;
  const unsigned sso = w == 4 ? WS_ML + kvh * 2048 + j * 16 : sh1;
  if (w < 4) {
    const int hh = (l >> 4) & 3;
    float mw[8], M = -INFINITY;
#pragma unroll
    for (int i = 0; i < 8; ++i) { mw[i] = sm.ml[0][hh][i][0]; M = fmaxf(M, mw[i]); }
    f32x2 t = {0.f, 0.f};
#pragma unroll
    for (int i = 0; i < 8; ++i) t += ((const f32x2*)&sm.acc[0][i][0])[tid] * ex2(mw[i] - M);
    const unsigned e0 = enc(t[0], f0, hi22), e1 = enc(t[1], f1, hi22);
    sv = u32x4{e0, e1, dppu<QP(1, 0, 3, 2)>(e0), dppu<QP(1, 0, 3, 2)>(e1)};
  } else if (w == 4) {
    const f32x2 v = sm.ml[(l >> 5) & 1][(l >> 3) & 3][l & 7];
    float M = v[0];
    M = fmaxf(M, dpp<QP(1, 0, 3, 2)>(M));
    M = fmaxf(M, dpp<QP(2, 3, 0, 1)>(M));
    M = fmaxf(M, dpp<0x141>(M));
    float L = v[1] * ex2(v[0] - M);
    L += dpp<QP(1, 0, 3, 2)>(L);
    L += dpp<QP(2, 3, 0, 1)>(L);
    L += dpp<0x141>(L);
    sv = u32x4{asu(M), tag, asu(L), tag};
    svo = (l & 7) ? DROP : (l >> 5) * 16384 + ((l >> 3) & 3) * 512;
  }
  st_sc1(sv, rws, svo, sso);
  SB;
  {
    f32x4 lo = {0.f, 0.f, 0.f, 0.f}, hi = lo;
    pv_group(vb, P[0], m0, m1, lo, hi);
#pragma unroll
    for (int G = 1; G < 4; ++G) pv_group(vb + 4 * G, P[G], m0, m1, lo, hi);
    sm.acc[1][w][2 * l] = lo * fB; sm.acc[1][w][2 * l + 1] = hi * fB;
  }
  asm volatile("" :: "v"(tc0), "v"(tc2));
  u32x4 gm, gd, wa[4];
  bar();
#define WPIN do { _Pragma("unroll") for (int i = 0; i < 2; ++i) { \
      wa[2 * i] = u32x4{wo[2 * i][0], wo[2 * i][1], wo[2 * i + 1][0], wo[2 * i + 1][1]}; \
      wa[2 * i + 1] = u32x4{wo[2 * i][2], wo[2 * i][3], wo[2 * i + 1][2], wo[2 * i + 1][3]}; } \
    asm volatile("" : "+v"(wa[0]), "+v"(wa[1]), "+v"(wa[2]), "+v"(wa[3])); } while (0)
  {
    u32x4 v = tv;
    if (w < 4) {
      f32x2 t[8];
#pragma unroll
      for (int i = 0; i < 8; ++i) t[i] = ((const f32x2*)&sm.acc[1][i][0])[tid];
      const f32x2 r = ((t[0] + t[1]) + (t[2] + t[3])) + ((t[4] + t[5]) + (t[6] + t[7]));
      const unsigned e0 = enc(r[0], f0, hi22), e1 = enc(r[1], f1, hi22);
      v = u32x4{e0, e1, dppu<QP(1, 0, 3, 2)>(e0), dppu<QP(1, 0, 3, 2)>(e1)};
    }
    st_sc1(v, rws, voh1, sh1b);
  }
  SB;
  if (w < 4) {
    WPIN;
  } else {
    gm = ld_sc1(rws, voml, soml);
    gd = ld_sc1(rws, voh, soh);
    SB;
    for (int it = 0; (gm[1] != tag || gm[3] != tag) && it < SPIN_CAP; ++it) {
      CLOB;
      gm = ld_sc1(rws, voml, soml);
    }
    const float mm = asf(gm[0]);
    float M = fmaxf(mm, dpp<ROR(1)>(mm));
    M = fmaxf(M, dpp<ROR(2)>(M));
    M = fmaxf(M, dpp<ROR(4)>(M));
    M = fmaxf(M, dpp<ROR(8)>(M));
    const float e = ex2(mm - M);
    float L = e * asf(gm[2]);
    L += dpp<ROR(1)>(L);
    L += dpp<ROR(2)>(L);
    L += dpp<ROR(4)>(L);
    L += dpp<ROR(8)>(L);
    const float el = e / L;
    const float wgt = asf((unsigned)__builtin_amdgcn_update_dpp((int)asu(dpp<NBC(0)>(el)), (int)asu(el), NBC(8), 0xf, 0xc, false));
    WPIN;
    SB;
    const f32x4 x = poll4(gd, tv, rws, voh, soh) * wgt;
    float v = add16(add32(x[0], x[2]), add32(x[1], x[3]));
    v += dpp<ROR(8)>(v);
    *po2w = v;
  }
  bar();
  {
    f32x2 t[4];
#pragma unroll
    for (int i = 0; i < 4; ++i) t[i] = *po2r[i];
    SB;
    unsigned x[8];
#pragma unroll
    for (int i = 0; i < 4; ++i) {
      const float o = t[i][0] + t[i][1];
      const unsigned pb = pk(o, o);
      x[2 * i] = pb & m0; x[2 * i + 1] = pb & m1;
    }
    f32x4 lo = {0.f, 0.f, 0.f, 0.f}, hi = lo;
    lo = mfma32(wa[0], u32x4{x[0], x[1], x[2], x[3]}, lo);
    hi = mfma32(wa[1], u32x4{x[0], x[1], x[2], x[3]}, hi);
    lo = mfma32(wa[2], u32x4{x[4], x[5], x[6], x[7]}, lo);
    hi = mfma32(wa[3], u32x4{x[4], x[5], x[6], x[7]}, hi);
    u32x4 r;
#pragma unroll
    for (int i = 0; i < 4; ++i) {
      const u32x2 t2 = sw32(lo[i], hi[i]);
      r[i] = enc(asf(t2[0]) + dpp<ROR(8)>(asf(t2[1])), tv[i], hi22);
    }
    st_sc1(r, rws, vh2, sh2);
  }
  SB;
  if (w < 4) {
    const f32x4 x = poll4(ld_sc1(rws, vov, sg2), tv, rws, vov, sg2);
    float v = add16(add32(x[0], x[2]), add32(x[1], x[3]));
    v += dpp<ROR(8)>(v);
    v += dpp<ROR(4)>(v);
    v += dpp<ROR(2)>(v);
    *po3w = v;
  }
  bar();
  if (w == 0) {
    const f32x4 a = sm.o3[l & 7];
    const float r = (a[0] + a[1]) + (a[2] + a[3]);
    __builtin_amdgcn_raw_buffer_store_b16((u16)pk(r, r), mk(out, 4096), vout, 0, 0);
    if (blk == 0) __builtin_amdgcn_raw_buffer_store_b32(tag, rws, l ? DROP : 0u, WS_EPOCH, AUX_SC1);
  }
}

extern "C" void kernel_launch(void* const* d_in, const int* in_sizes, int n_in, void* d_out, int out_size, void* d_ws,
                              size_t ws_size, hipStream_t stream) {
  fused_decode<<<256, 512, 0, stream>>>(KARGS(d_in, d_out, d_ws));
}
```

```cpp
#include <hip/hip_runtime.h>
#include <hip/hip_bf16.h>
#include <stdint.h>

typedef unsigned char u8;
typedef unsigned short u16;
typedef __attribute__((ext_vector_type(4))) unsigned u32x4;
typedef __attribute__((ext_vector_type(2))) unsigned u32x2;
typedef __attribute__((ext_vector_type(4))) float f32x4;
typedef __attribute__((ext_vector_type(2))) float f32x2;
typedef __attribute__((ext_vector_type(8))) __bf16 bf16x8;
typedef __attribute__((ext_vector_type(2))) __bf16 bf16x2;
typedef __attribute__((ext_vector_type(4))) short s16x4;
typedef __amdgpu_buffer_rsrc_t rsrc_t;
#define LDS __attribute__((address_space(3)))

#define SB __builtin_amdgcn_sched_barrier(0)
#define CLOB asm volatile("" ::: "memory")
#define DEVI __device__ __forceinline__

#define WS_EPOCH 0u
#define WS_Q     4096u
#define WS_ML    (WS_Q + 524288u)
#define WS_H1    (WS_ML + 32768u)
#define WS_H2    (WS_H1 + 1048576u)
#define WS_END   (WS_H2 + 1048576u)
#define SPIN_CAP 100000

#define DROP 0x80000000u
#define AUX_NT  2
#define AUX_SC1 16

DEVI rsrc_t mk(const void* p, unsigned bytes) {
  return __builtin_amdgcn_make_buffer_rsrc((void*)p, (short)0, (int)bytes, 0x00020000);
}
DEVI u32x4 ld_nt(rsrc_t r, unsigned voff, unsigned soff) {
  return __builtin_amdgcn_raw_buffer_load_b128(r, (int)voff, (int)soff, AUX_NT);
}
DEVI u32x4 ld_sc1(rsrc_t r, unsigned voff, unsigned soff) {
  return __builtin_amdgcn_raw_buffer_load_b128(r, (int)voff, (int)soff, AUX_SC1);
}
DEVI void st_sc1(u32x4 v, rsrc_t r, unsigned voff, unsigned soff) {
  __builtin_amdgcn_raw_buffer_store_b128(v, r, (int)voff, (int)soff, AUX_SC1);
}
DEVI float asf(unsigned v) { return __uint_as_float(v); }
DEVI unsigned asu(float v) { return __float_as_uint(v); }
DEVI float blo(unsigned v) { return asf(v << 16); }
DEVI float bhi(unsigned v) { return asf(v & 0xffff0000u); }
DEVI unsigned pk(float a, float b) {
  f32x2 f = {a, b};
  bf16x2 h = __builtin_convertvector(f, bf16x2);
  return __builtin_bit_cast(unsigned, h);
}
template <int CTRL> DEVI float dpp(float v) {
  return asf((unsigned)__builtin_amdgcn_update_dpp(0, (int)asu(v), CTRL, 0xf, 0xf, true));
}
template <int CTRL> DEVI unsigned dppu(unsigned v) {
  return (unsigned)__builtin_amdgcn_update_dpp(0, (int)v, CTRL, 0xf, 0xf, true);
}
#define QP(a, b, c, d) ((a) | ((b) << 2) | ((c) << 4) | ((d) << 6))
#define ROR(n) (0x120 + (n))
#define NBC(n) (0x150 + (n))
DEVI u32x2 sw32(float a, float b) { auto r = __builtin_amdgcn_permlane32_swap(asu(a), asu(b), false, false); return u32x2{r[0], r[1]}; }
DEVI u32x2 sw16(float a, float b) { auto r = __builtin_amdgcn_permlane16_swap(asu(a), asu(b), false, false); return u32x2{r[0], r[1]}; }
DEVI float add32(float a, float b) { u32x2 r = sw32(a, b); return asf(r[0]) + asf(r[1]); }
DEVI float add16(float a, float b) { u32x2 r = sw16(a, b); return asf(r[0]) + asf(r[1]); }
DEVI float max32(float a) { u32x2 r = sw32(a, a); return fmaxf(asf(r[0]), asf(r[1])); }
DEVI float max16(float a) { u32x2 r = sw16(a, a); return fmaxf(asf(r[0]), asf(r[1])); }
DEVI float ex2(float x) { return __builtin_amdgcn_exp2f(x); }
DEVI f32x4 mfma32(u32x4 a, u32x4 b, f32x4 c) {
  return __builtin_amdgcn_mfma_f32_16x16x32_bf16(__builtin_bit_cast(bf16x8, a), __builtin_bit_cast(bf16x8, b), c, 0, 0, 0);
}
DEVI f32x4 mfma16(unsigned a0, unsigned a1, unsigned b0, unsigned b1, f32x4 c) {
  u32x2 a = {a0, a1}, b = {b0, b1};
  return __builtin_amdgcn_mfma_f32_16x16x16bf16_1k(__builtin_bit_cast(s16x4, a), __builtin_bit_cast(s16x4, b), c, 0, 0, 0);
}
DEVI void bar() { __builtin_amdgcn_fence(__ATOMIC_RELEASE, "workgroup", "local"); __builtin_amdgcn_s_barrier(); __builtin_amdgcn_fence(__ATOMIC_ACQUIRE, "workgroup", "local"); }

DEVI float qk_group(const u32x4* k, const u32x4* qf, bool odd) {
  f32x4 d = {0.f, 0.f, 0.f, 0.f};
  d = mfma32(k[0], qf[0], d);
  d = mfma32(k[1], qf[1], d);
  d = mfma32(k[2], qf[2], d);
  d = mfma32(k[3], qf[3], d);
  float a = odd ? d[3] : d[0], b = odd ? d[1] : d[2];
  float u = a + dpp<QP(1, 0, 3, 2)>(b);
  u = blo(pk(u, u)) * 0.08838834764831845f;
  return blo(pk(u, u));
}
DEVI float head_max(float m) {
  m = fmaxf(m, dpp<QP(1, 0, 3, 2)>(m));
  m = fmaxf(m, dpp<QP(2, 3, 0, 1)>(m));
  return max32(max16(m));
}
DEVI float head_sum(float m) {
  m += dpp<QP(1, 0, 3, 2)>(m);
  m += dpp<QP(2, 3, 0, 1)>(m);
  m = add16(m, m);
  return add32(m, m);
}
template <int I> DEVI void pv_one(u32x4 v, unsigned P, unsigned m0, unsigned m1, f32x4& lo, f32x4& hi) {
  unsigned pb = dppu<QP(I, I, I, I)>(P);
  unsigned x0 = pb & m0, x1 = pb & m1;
  lo = mfma16(v[0], v[1], x0, x1, lo);
  hi = mfma16(v[2], v[3], x0, x1, hi);
}
DEVI void pv_group(const u32x4* v, unsigned P, unsigned m0, unsigned m1, f32x4& lo, f32x4& hi) {
  pv_one<0>(v[0], P, m0, m1, lo, hi);
  pv_one<1>(v[1], P, m0, m1, lo, hi);
  pv_one<2>(v[2], P, m0, m1, lo, hi);
  pv_one<3>(v[3], P, m0, m1, lo, hi);
}

DEVI unsigned enc(float v, unsigned f, unsigned hi) { return __builtin_amdgcn_bitop3_b32(asu(v) + 0x200u, hi, f, 0xea); }
DEVI unsigned enc8(float v, unsigned f) { return ((asu(v) + 0x80u) & ~0xffu) | f; }
DEVI f32x4 poll4(u32x4 g, u32x4 f, rsrc_t r, unsigned voff, unsigned soff) {
  u32x4 x = g ^ f;
  for (int it = 0; ((x[0] | x[1] | x[2] | x[3]) & 0x3ffu) != 0 && it < SPIN_CAP; ++it) {
    CLOB;
    x = ld_sc1(r, voff, soff) ^ f;
  }
  return __builtin_bit_cast(f32x4, x);
}

#define KARGS(d, o, w) (const u8*)d[3], (const u8*)d[8], (const u8*)d[0], (const u8*)d[5], (u8*)w, (const u8*)d[1], (const u8*)d[2], \
                       (const u8*)d[6], (const u8*)d[7], (const u8*)d[4], (const u8*)d[9], (u8*)o
struct Smem {
  f32x4 qred[8][64];
  unsigned qs[2][4][64];
  f32x4 acc[2][8][128];
  f32x2 ml[2][4][8];
  float o2[2][80];
  f32x4 o3[8];
};

__global__ void __launch_bounds__(512, 2)
fused_decode(const u8* __restrict__ wqA, const u8* __restrict__ wqB, const u8* __restrict__ xA, const u8* __restrict__ xB, u8* ws,
             const u8* __restrict__ kA, const u8* __restrict__ vA, const u8* __restrict__ kB, const u8* __restrict__ vB,
             const u8* __restrict__ woA, const u8* __restrict__ woB, u8* __restrict__ out) {
  __shared__ Smem sm;
  const int tid = threadIdx.x, l = tid & 63, w = __builtin_amdgcn_readfirstlane(tid >> 6);
  const int blk = blockIdx.x, kvh = blk & 7, j = blk >> 3,
            b = j >> 4, s = j & 15, g = b * 8 + kvh;
  const float LOG2E = 1.4426950408889634f;
  const int kq = s >> 1, hq = ((s & 1) << 1) | ((kvh & 1) ^ 1), qq = kvh >> 1;
  unsigned vo = (l >> 4) * 32768 + (l & 15) * 16, so = qq * 1048576 + w * 131072 + kq * 1024 + hq * 256;
  unsigned vx = (l >> 4) * 8, sx = qq * 256 + w * 32, so1 = so + 8192, so2 = so + 16384, so3 = so + 24576;
  asm volatile("" : "+v"(vo), "+v"(vx), "+s"(so), "+s"(so1), "+s"(so2), "+s"(so3), "+s"(sx));
  SB;
  asm volatile("" :: "s"(wqA), "s"(wqB), "s"(xA), "s"(xB), "s"(ws), "s"(kA), "s"(vA), "s"(kB), "s"(vB), "s"(woA), "s"(woB), "s"(out));
  const rsrc_t rwq = mk(b ? wqB : wqA, 4194304);
  const rsrc_t rx = mk(b ? xB : xA, 2048);
  u32x4 wq[4], ka[4], va[4], kb[16], vb[16], wo[4];
  u32x2 x0, x1;
  x0 = __builtin_amdgcn_raw_buffer_load_b64(rx, vx, sx, 0);
  x1 = __builtin_amdgcn_raw_buffer_load_b64(rx, vx + 1024, sx, 0);
  SB;
  wq[0] = ld_nt(rwq, vo, so); SB;
  wq[1] = ld_nt(rwq, vo, so1); SB;
  wq[2] = ld_nt(rwq, vo, so2); SB;
  wq[3] = ld_nt(rwq, vo, so3); SB;
  unsigned ep;
  asm volatile("s_load_dword %0, %1, 0x0" : "=&s"(ep) : "s"(ws) : "memory");
  SB;
  const int bc = kvh >> 2, kc = ((kvh & 4) ^ (b * 4)) | ((kvh + 1 - b) & 3), ci = (kvh & 3) * 32 + j;
  const rsrc_t rws = mk(ws, WS_END);
  const rsrc_t rwo = mk((bc ? woB : woA) + (size_t)(kc * 512 + s * 32) * 1024, 32768);
  const rsrc_t rkA = mk(kA + ((size_t)g * 8192 + 6144) * 256, 524288);
  const rsrc_t rvA = mk(vA + ((size_t)g * 8192 + 6144) * 256, 524288);
  const rsrc_t rkB = mk(kB + (size_t)g * 8192 * 256, 2097152);
  const rsrc_t rvB = mk(vB + (size_t)g * 8192 * 256, 2097152);
  const unsigned vok = (4 * ((l >> 1) & 1) + ((l >> 2) & 3)) * 256 + (l >> 4) * 32 + (l & 1) * 16, vov = l * 16;
#define SOA(t) ((unsigned)((s * 16 + w * 2 + (t) + 17 * g) & 255) * 2048u)
#define SOB(t) ((unsigned)(((((w >> 2) * 2 + ((t) >> 2) + s) & 3) * 256) + ((s * 16 + (w & 3) * 4 + ((t) & 3) + 65 * g) & 255)) * 2048u)
  __builtin_amdgcn_s_barrier();
  SB;
#pragma unroll
  for (int i = 0; i < 4; ++i) { ka[i] = ld_nt(rkA, vok + (i & 1) * 128, SOA(i >> 1)); SB; }
#pragma unroll
  for (int i = 0; i < 4; ++i) { va[i] = ld_nt(rvA, vov + (i & 1) * 1024, SOA(i >> 1)); SB; }
#define LDK(a, e) _Pragma("unroll") for (int i = a; i < e; ++i) { kb[i] = ld_nt(rkB, vok + (i & 1) * 128, SOB(i >> 1)); SB; }
#define LDV(a, e) _Pragma("unroll") for (int i = a; i < e; ++i) { vb[i] = ld_nt(rvB, vov + (i & 1) * 1024, SOB(i >> 1)); SB; }
  LDK(0, 2)
  {
    float a[16];
#pragma unroll
    for (int i = 0; i < 16; ++i) a[i] = 0.f;
#pragma unroll
    for (int i = 0; i < 4; ++i) {
      const float xa = (i & 1) ? bhi(x0[i >> 1]) : blo(x0[i >> 1]), xb = (i & 1) ? bhi(x1[i >> 1]) : blo(x1[i >> 1]);
#pragma unroll
      for (int c = 0; c < 4; ++c) {
        const float e0 = blo(wq[i][c]), e1 = bhi(wq[i][c]);
        a[2 * c] = fmaf(e0, xa, a[2 * c]);
        a[2 * c + 1] = fmaf(e1, xa, a[2 * c + 1]);
        a[8 + 2 * c] = fmaf(e0, xb, a[8 + 2 * c]);
        a[8 + 2 * c + 1] = fmaf(e1, xb, a[8 + 2 * c + 1]);
      }
    }
#pragma unroll
    for (int i = 0; i < 8; ++i) a[i] = add32(a[i], a[i + 8]);
#pragma unroll
    for (int i = 0; i < 4; ++i) a[i] = add16(a[i], a[i + 4]);
    sm.qred[w][l] = f32x4{a[0], a[1], a[2], a[3]};
  }
  asm volatile("s_waitcnt lgkmcnt(0)" : "+s"(ep)::"memory");
  const unsigned tag = ep + 0x01010101u;
  const u32x4 tf = {tag & 0x3ffu, (tag >> 8) & 0x3ffu, (tag >> 16) & 0x3ffu, ((tag >> 24) | (tag << 8)) & 0x3ffu}, tq = tf & 0xffu;

  bar();
  if (w < 2) {
    f32x2 t[8];
#pragma unroll
    for (int i = 0; i < 8; ++i) t[i] = ((const f32x2*)&sm.qred[i][0])[tid];
    const f32x2 r = ((t[0] + t[1]) + (t[2] + t[3])) + ((t[4] + t[5]) + (t[6] + t[7]));
    const unsigned e0 = enc8(r[0], (l & 1) ? tq[2] : tq[0]), e1 = enc8(r[1], (l & 1) ? tq[3] : tq[1]);
    st_sc1(u32x4{e0, e1, dppu<QP(1, 0, 3, 2)>(e0), dppu<QP(1, 0, 3, 2)>(e1)}, rws, (l & 1) ? DROP : (l & 30) * 16 + (l >> 5) * 16,
           WS_Q + b * 131072 + w * 65536 + kq * 8192 + (hq * 4 + qq) * 512);
  }
  SB;
  LDK(2, 16)
  const bool odd = l & 1;
  u32x4 tv = tf;
  unsigned hi22 = ~0x3ffu, f0 = odd ? tf[2] : tf[0], f1 = odd ? tf[3] : tf[1];
  unsigned voh1 = (odd || w >= 4) ? DROP : (l >> 4) * 16384 + (l & 15) * 8, sh1 = WS_H1 + kvh * 65536 + w * 4096 + j * 128, sh1b = sh1 + 524288;
  unsigned soml = WS_ML + bc * 16384 + kc * 2048 + (s >> 2) * 512 + ((w >> 1) & 1) * 256;
  unsigned voml = (((l & 15) + (l >> 4) + 4 * (w & 1)) & 15) * 16;
  unsigned soh = WS_H1 + bc * 524288 + kc * 65536 + s * 4096 + ((w >> 1) & 1) * 2048 + (w & 1) * 512;
  unsigned voh = ((l >> 4) + (l & 8)) * 128 + (l & 7) * 16;
  LDS float* po2w = (LDS float*)&sm.o2[(w >> 1) & 1][(8 * (l >> 4) + 4 * (l & 1) + ((l >> 1) & 3)) * 2 + (w & 1)];
  const LDS f32x2* po2r[4];
#pragma unroll
  for (int i = 0; i < 4; ++i) po2r[i] = (const LDS f32x2*)&sm.o2[(l >> 2) & 1][(((i + s) & 3) * 8 + (l >> 4) * 2 + ((l >> 3) & 1)) * 2];
  unsigned vh2 = (l & 8) ? DROP : ((l >> 4) * 4 + ((l >> 1) & 3)) * 4096 + (l & 1) * 16;
  unsigned sh2 = WS_H2 + bc * 524288 + w * 65536 + (kc * 16 + s) * 32, sg2 = WS_H2 + (bc ^ 1) * 524288 + ci * 4096 + (w & 3) * 1024;
  LDS float* po3w = (LDS float*)&sm.o3[(l >> 4) * 2 + (l & 1)] + (w & 3);
  unsigned vout = l >= 8 ? DROP : (((ci >> 1) & 1) * 1024 + (bc ^ 1) * 512 + 64 * (ci >> 4) + 32 * ((ci >> 2) & 1) + 8 * (l >> 1) + 4 * ((ci >> 3) & 1) + 2 * (ci & 1) + (l & 1)) * 2;
  {
    const unsigned soq = WS_Q + (w >> 2) * 131072 + b * 65536 + kvh * 8192 + (w & 3) * 2048;
    u32x4 g0 = ld_sc1(rws, l * 16, soq);
    SB;
    u32x4 g1 = ld_sc1(rws, l * 16 + 1024, soq);
    SB;
    LDV(0, 4)
    asm volatile("" : "+v"(f0), "+v"(f1), "+v"(voh1), "+v"(voml), "+v"(voh), "+v"(vh2), "+v"(vout), "+v"(po2w), "+v"(po3w));
    asm volatile("" : "+v"(po2r[0]), "+v"(po2r[1]), "+v"(po2r[2]), "+v"(po2r[3]), "+v"(tv));
    asm volatile("" : "+s"(hi22), "+s"(sh1), "+s"(sh1b), "+s"(soml), "+s"(soh), "+s"(sh2), "+s"(sg2));
    SB;
#define QBAD (((g0[0] | g0[1] | g0[2] | g0[3] | g1[0] | g1[1] | g1[2] | g1[3]) & 0xffu) != 0)
    g0 ^= tq; g1 ^= tq;
    if (__any(QBAD)) {
      for (int it = 0; it < SPIN_CAP; ++it) {
        CLOB;
        g0 = ld_sc1(rws, l * 16, soq) ^ tq;
        g1 = ld_sc1(rws, l * 16 + 1024, soq) ^ tq;
        if (!__any(QBAD)) break;
      }
    }
    const f32x4 x = __builtin_bit_cast(f32x4, g0) + __builtin_bit_cast(f32x4, g1);
    sm.qs[w >> 2][w & 3][2 * (l & 31) + (l >> 5)] = pk(add32(x[0], x[2]), add32(x[1], x[3]));
  }
  SB;
  bar();
  LDV(4, 12)

  const int h = (l >> 2) & 3;
  const unsigned m0 = (l & 2) ? 0u : (odd ? 0xffff0000u : 0xffffu), m1 = (l & 2) ? (odd ? 0xffff0000u : 0xffffu) : 0u;
  const unsigned t0 = (l & 2) ? 0u : ~0u;
  u32x4 qf[4];
  {
    const u32x4 q0 = *(const u32x4*)&sm.qs[0][h][(l >> 4) * 8 + (l & 1) * 4], q1 = *(const u32x4*)&sm.qs[0][h][32 + (l >> 4) * 8 + (l & 1) * 4];
    qf[0] = q0 & t0; qf[1] = q1 & t0; qf[2] = q0 & ~t0; qf[3] = q1 & ~t0;
    const float u = qk_group(ka, qf, odd);
    const float m2 = head_max(u) * LOG2E;
    const float p = ex2(fmaf(u, LOG2E, -m2));
    const float ls = head_sum(p);
    f32x4 lo = {0.f, 0.f, 0.f, 0.f}, hi = lo;
    pv_group(va, pk(p, p), m0, m1, lo, hi);
    sm.acc[0][w][2 * l] = lo; sm.acc[0][w][2 * l + 1] = hi;
    if ((l & 0x33) == 0) sm.ml[0][h][w] = f32x2{m2, ls};
  }
  SB;
  LDV(12, 16)
  unsigned P[4];
  float m2B;
  {
    const u32x4 q0 = *(const u32x4*)&sm.qs[1][h][(l >> 4) * 8 + (l & 1) * 4], q1 = *(const u32x4*)&sm.qs[1][h][32 + (l >> 4) * 8 + (l & 1) * 4];
    qf[0] = q0 & t0; qf[1] = q1 & t0; qf[2] = q0 & ~t0; qf[3] = q1 & ~t0;
    float u[4];
    u[0] = qk_group(kb, qf, odd);
    u[1] = qk_group(kb + 4, qf, odd);
    u[2] = qk_group(kb + 8, qf, odd);
    u[3] = qk_group(kb + 12, qf, odd);
    m2B = head_max(fmaxf(fmaxf(u[0], u[1]), fmaxf(u[2], u[3]))) * LOG2E;
    float ls = 0.f;
#pragma unroll
    for (int i = 0; i < 4; ++i) { const float p = ex2(fmaf(u[i], LOG2E, -m2B)); ls += p; P[i] = pk(p, p); }
    ls = head_sum(ls);
    if ((l & 0x33) == 0) sm.ml[1][h][w] = f32x2{m2B, ls};
  }
  bar();
  const unsigned vw = (l >> 3) * 1024 + (l & 7) * 16;
#pragma unroll
  for (int i = 0; i < 4; ++i) { wo[i] = ld_nt(rwo, vw, ((i + s) & 3) * 8192 + w * 128); SB; }
  float fB;
  {
    const f32x4* mp = (const f32x4*)&sm.ml[1][h][0];
    const f32x4 a0 = mp[0], a1 = mp[1], a2 = mp[2], a3 = mp[3];
    const float M = fmaxf(fmaxf(fmaxf(a0[0], a0[2]), fmaxf(a1[0], a1[2])), fmaxf(fmaxf(a2[0], a2[2]), fmaxf(a3[0], a3[2])));
    fB = ex2(m2B - M);
  }
  u32x4 sv = tv;
  unsigned svo = voh1;
  const unsigned sso = w == 4 ? WS_ML + kvh * 2048 + j * 16 : sh1;
  if (w < 4) {
    const int hh = (l >> 4) & 3;
    float mw[8], M = -INFINITY;
#pragma unroll
    for (int i = 0; i < 8; ++i) { mw[i] = sm.ml[0][hh][i][0]; M = fmaxf(M, mw[i]); }
    f32x2 t = {0.f, 0.f};
#pragma unroll
    for (int i = 0; i < 8; ++i) t += ((const f32x2*)&sm.acc[0][i][0])[tid] * ex2(mw[i] - M);
    const unsigned e0 = enc(t[0], f0, hi22), e1 = enc(t[1], f1, hi22);
    sv = u32x4{e0, e1, dppu<QP(1, 0, 3, 2)>(e0), dppu<QP(1, 0, 3, 2)>(e1)};
  } else if (w == 4) {
    const f32x2 v = sm.ml[(l >> 5) & 1][(l >> 3) & 3][l & 7];
    float M = v[0];
    M = fmaxf(M, dpp<QP(1, 0, 3, 2)>(M));
    M = fmaxf(M, dpp<QP(2, 3, 0, 1)>(M));
    M = fmaxf(M, dpp<0x141>(M));
    float L = v[1] * ex2(v[0] - M);
    L += dpp<QP(1, 0, 3, 2)>(L);
    L += dpp<QP(2, 3, 0, 1)>(L);
    L += dpp<0x141>(L);
    sv = u32x4{asu(M), tag, asu(L), tag};
    svo = (l & 7) ? DROP : (l >> 5) * 16384 + ((l >> 3) & 3) * 512;
  }
  st_sc1(sv, rws, svo, sso);
  SB;
  {
    f32x4 lo = {0.f, 0.f, 0.f, 0.f}, hi = lo;
    pv_group(vb, P[0], m0, m1, lo, hi);
#pragma unroll
    for (int G = 1; G < 4; ++G) pv_group(vb + 4 * G, P[G], m0, m1, lo, hi);
    sm.acc[1][w][2 * l] = lo * fB; sm.acc[1][w][2 * l + 1] = hi * fB;
  }
  u32x4 gm, gd, wa[4];
  bar();
#define WPIN do { _Pragma("unroll") for (int i = 0; i < 2; ++i) { \
      wa[2 * i] = u32x4{wo[2 * i][0], wo[2 * i][1], wo[2 * i + 1][0], wo[2 * i + 1][1]}; \
      wa[2 * i + 1] = u32x4{wo[2 * i][2], wo[2 * i][3], wo[2 * i + 1][2], wo[2 * i + 1][3]}; } \
    asm volatile("" : "+v"(wa[0]), "+v"(wa[1]), "+v"(wa[2]), "+v"(wa[3])); } while (0)
  {
    u32x4 v = tv;
    if (w < 4) {
      f32x2 t[8];
#pragma unroll
      for (int i = 0; i < 8; ++i) t[i] = ((const f32x2*)&sm.acc[1][i][0])[tid];
      const f32x2 r = ((t[0] + t[1]) + (t[2] + t[3])) + ((t[4] + t[5]) + (t[6] + t[7]));
      const unsigned e0 = enc(r[0], f0, hi22), e1 = enc(r[1], f1, hi22);
      v = u32x4{e0, e1, dppu<QP(1, 0, 3, 2)>(e0), dppu<QP(1, 0, 3, 2)>(e1)};
    }
    st_sc1(v, rws, voh1, sh1b);
  }
  SB;
  if (w < 4) {
    WPIN;
  } else {
    gm = ld_sc1(rws, voml, soml);
    gd = ld_sc1(rws, voh, soh);
    SB;
    for (int it = 0; (gm[1] != tag || gm[3] != tag) && it < SPIN_CAP; ++it) {
      CLOB;
      gm = ld_sc1(rws, voml, soml);
    }
    const float mm = asf(gm[0]);
    float M = fmaxf(mm, dpp<ROR(1)>(mm));
    M = fmaxf(M, dpp<ROR(2)>(M));
    M = fmaxf(M, dpp<ROR(4)>(M));
    M = fmaxf(M, dpp<ROR(8)>(M));
    const float e = ex2(mm - M);
    float L = e * asf(gm[2]);
    L += dpp<ROR(1)>(L);
    L += dpp<ROR(2)>(L);
    L += dpp<ROR(4)>(L);
    L += dpp<ROR(8)>(L);
    const float el = e / L;
    const float wgt = asf((unsigned)__builtin_amdgcn_update_dpp((int)asu(dpp<NBC(0)>(el)), (int)asu(el), NBC(8), 0xf, 0xc, false));
    WPIN;
    SB;
    const f32x4 x = poll4(gd, tv, rws, voh, soh) * wgt;
    float v = add16(add32(x[0], x[2]), add32(x[1], x[3]));
    v += dpp<ROR(8)>(v);
    *po2w = v;
  }
  bar();
  {
    f32x2 t[4];
#pragma unroll
    for (int i = 0; i < 4; ++i) t[i] = *po2r[i];
    SB;
    unsigned x[8];
#pragma unroll
    for (int i = 0; i < 4; ++i) {
      const float o = t[i][0] + t[i][1];
      const unsigned pb = pk(o, o);
      x[2 * i] = pb & m0; x[2 * i + 1] = pb & m1;
    }
    f32x4 lo = {0.f, 0.f, 0.f, 0.f}, hi = lo;
    lo = mfma32(wa[0], u32x4{x[0], x[1], x[2], x[3]}, lo);
    hi = mfma32(wa[1], u32x4{x[0], x[1], x[2], x[3]}, hi);
    lo = mfma32(wa[2], u32x4{x[4], x[5], x[6], x[7]}, lo);
    hi = mfma32(wa[3], u32x4{x[4], x[5], x[6], x[7]}, hi);
    u32x4 r;
#pragma unroll
    for (int i = 0; i < 4; ++i) {
      const u32x2 t2 = sw32(lo[i], hi[i]);
      r[i] = enc(asf(t2[0]) + dpp<ROR(8)>(asf(t2[1])), tv[i], hi22);
    }
    st_sc1(r, rws, vh2, sh2);
  }
  SB;
  if (w < 4) {
    const f32x4 x = poll4(ld_sc1(rws, vov, sg2), tv, rws, vov, sg2);
    float v = add16(add32(x[0], x[2]), add32(x[1], x[3]));
    v += dpp<ROR(8)>(v);
    v += dpp<ROR(4)>(v);
    v += dpp<ROR(2)>(v);
    *po3w = v;
  }
  bar();
  if (w == 0) {
    const f32x4 a = sm.o3[l & 7];
    const float r = (a[0] + a[1]) + (a[2] + a[3]);
    __builtin_amdgcn_raw_buffer_store_b16((u16)pk(r, r), mk(out, 4096), vout, 0, 0);
    if (blk == 0) __builtin_amdgcn_raw_buffer_store_b32(tag, rws, l ? DROP : 0u, WS_EPOCH, AUX_SC1);
  }
}

extern "C" void kernel_launch(void* const* d_in, const int* in_sizes, int n_in, void* d_out, int out_size, void* d_ws,
                              size_t ws_size, hipStream_t stream) {
  fused_decode<<<256, 512, 0, stream>>>(KARGS(d_in, d_out, d_ws));
}
```
